```python
import torch

def _softmax(x, axis=-1):
    return torch.softmax(x, dim=axis)
def _gelu(x):
    return torch.nn.functional.gelu(x, approximate="tanh")
def _silu(x):
    return torch.nn.functional.silu(x)
def _sigmoid(x):
    return torch.sigmoid(x)
def _layernorm(x, axis=-1, eps=1e-5):
    m = x.mean(dim=axis, keepdim=True)
    v = x.var(dim=axis, keepdim=True, unbiased=False)
    return (x - m) / torch.sqrt(v + eps)
def _scatter_add(tgt, idx, src, axis=0):
    return tgt.clone().index_add_(axis, idx, src)
def _segment_sum(src, seg, n):
    out = torch.zeros((n,) + src.shape[1:], dtype=src.dtype)
    return out.index_add_(0, seg, src)
def _pad(x, pw):
    flat = tuple(v for pair in reversed(pw) for v in pair)
    return torch.nn.functional.pad(x, flat)

kernel_name = 'concat_heads__ssm_conv_h3__sliding_block__prompt'

def setup_inputs():

    _w = (lambda a: (lambda t: t.to(torch.bfloat16) if t.is_floating_point() else t)(torch.as_tensor(a)))
    def _A_setup():
        B, S, D = 1, 2048, 1024
        ks = 4
        return {
            "q": _w((lambda *s: torch.randn(*s, dtype=torch.bfloat16) * 0.05)(B, S, D)),
            "k": _w((lambda *s: torch.randn(*s, dtype=torch.bfloat16) * 1.6)(B, S, D)),
            "v": _w((lambda *s: torch.randn(*s, dtype=torch.bfloat16) * 0.8)(B, S, D)),
            "shift_k": _w((lambda *s: torch.randn(*s, dtype=torch.bfloat16) * 1.6)(D, ks) * 0.5),
            "ssm_k": _w((lambda *s: torch.randn(*s, dtype=torch.bfloat16) * 1.6)(D, S) * (1.0 / S ** 0.5)),
            "ssm_D": _w((lambda *s: torch.randn(*s, dtype=torch.bfloat16) * 0.8)(D)),
        }
    _A_in = _A_setup()

    def _B_setup():
        B, S, D = 1, 2048, 1024
        return {
            "x": _w((lambda *s: torch.randn(*s, dtype=torch.bfloat16) * 0.2)(B, S, D)),
            "wqkv": _w((lambda *s: torch.randn(*s, dtype=torch.bfloat16) * 0.2)(D, 3 * D)),
            "wo": _w((lambda *s: torch.randn(*s, dtype=torch.bfloat16) * 0.05)(D, D)),
        }
    _B_in = _B_setup()

    return {
        "A_q": _A_in["q"],
        "A_k": _A_in["k"],
        "A_v": _A_in["v"],
        "A_shift_k": _A_in["shift_k"],
        "A_ssm_k": _A_in["ssm_k"],
        "A_ssm_D": _A_in["ssm_D"],
        "B_x": _B_in["x"],
        "B_wqkv": _B_in["wqkv"],
        "B_wo": _B_in["wo"],
    }

def reference(A_q, A_k, A_v, A_shift_k, A_ssm_k, A_ssm_D, B_x, B_wqkv, B_wo):
    def _A_ref(q, k, v, shift_k, ssm_k, ssm_D):
        B, S, D = q.shape
        ks = shift_k.shape[-1]
        k_pad = _pad(k, ((0, 0), (ks - 1, 0), (0, 0)))
        k_shift = torch.zeros_like(k)
        for t in range(ks):
            k_shift = k_shift + k_pad[:, t:t + S, :] * shift_k[:, ks - 1 - t][None, None, :]
        kv = k_shift * v
        L = 2 * S
        kv_pad = _pad(kv, ((0, 0), (0, S), (0, 0)))
        kf_pad = _pad(ssm_k, ((0, 0), (0, S)))
        kv_f = (lambda x, axes=(-2,-1): torch.fft.rfft2(x.float(), dim=axes))(kv_pad[:, None, :, :], axes=(1, 2))
        kf_f = (lambda x, axes=(-2,-1): torch.fft.rfft2(x.float(), dim=axes))(kf_pad[:, None, :], axes=(1, 2))
        kf_f = (lambda x, *d: x.permute(*d))(kf_f, 1, 2, 0)
        y_f = kv_f * kf_f[None, :, :, :]
        y = (lambda x, s, axes=(-2,-1): torch.fft.irfft2(x, s=s, dim=axes).to(torch.bfloat16))(y_f, (1, L), axes=(1, 2))[:, 0, :S, :]
        y = y + kv * ssm_D[None, None, :]
        return q * y

    def _B_ref(x, wqkv, wo):
        B, S, D = x.shape
        H = 32; Dh = D // H; W = 256
        qkv = (lambda x, *d: x.permute(*d))((x @ wqkv).reshape(B, S, 3, H, Dh), 2, 0, 3, 1, 4)
        q, k, v = qkv[0], qkv[1], qkv[2]
        scores = torch.einsum("bhqd,bhkd->bhqk", q, k) * (Dh ** -0.5)
        idx = torch.arange(S)
        mask = (idx[:, None] - idx[None, :] >= 0) & (idx[:, None] - idx[None, :] < W)
        scores = torch.where(mask[None, None], scores, -1e9)
        p = _softmax(scores, axis=-1)
        o = torch.einsum("bhqk,bhkd->bhqd", p, v)
        return (lambda x, *d: x.permute(*d))(o, 0, 2, 1, 3).reshape(B, S, D) @ wo

    a = _A_ref(A_q, A_k, A_v, A_shift_k, A_ssm_k, A_ssm_D)
    b = _B_ref(B_x, B_wqkv, B_wo)
    return torch.concatenate([a, b], axis=-1)
```

```cpp
#include <hip/hip_runtime.h>
#include <hip/hip_bf16.h>
typedef unsigned short u16;
__device__ __forceinline__ float b2f(u16 x){ return __uint_as_float(((unsigned)x)<<16); }
__device__ __forceinline__ u16 f2b(float x){ unsigned u=__float_as_uint(x); u += 0x7fffu + ((u>>16)&1u); return (u16)(u>>16); }
__device__ __forceinline__ float rb(float x){ return b2f(f2b(x)); }
constexpr int S=2048, D=1024;

__global__ void k_kv(const u16* k, const u16* v, const u16* sk, u16* kv){
  int i = blockIdx.x*256+threadIdx.x; int s=i/D, d=i%D;
  float acc=0.f;
  for(int t=0;t<4;++t){ int j=3-t; float kk = (s-j>=0)? b2f(k[(s-j)*D+d]) : 0.f; acc = rb(acc + rb(kk*b2f(sk[d*4+j]))); }
  kv[i]=f2b(acc*b2f(v[i]));
}
__global__ void k_conv(const u16* kv, const u16* h, const u16* Dv, const u16* q, u16* out){
  int i = blockIdx.x*256+threadIdx.x; int s=i/D, d=i%D;
  float acc=0.f;
  for(int t=0;t<=s;++t) acc += b2f(h[d*S+t])*b2f(kv[(s-t)*D+d]);
  float y = rb(acc);
  y = rb(y + rb(b2f(kv[i])*b2f(Dv[d])));
  out[s*2048+d]=f2b(b2f(q[i])*y);
}
__global__ void k_gemm(const u16* A, const u16* B, u16* C, int N, int ldc){
  int n = blockIdx.x*256+threadIdx.x; int m = blockIdx.y;
  float acc=0.f;
  for(int k=0;k<1024;++k) acc += b2f(A[m*1024+k])*b2f(B[k*N+n]);
  C[m*ldc+n]=f2b(acc);
}
__global__ void k_attn(const u16* qkv, u16* o){
  int i = blockIdx.x*256+threadIdx.x; int s=i/32, h=i%32;
  float q[32];
  #pragma unroll
  for(int d=0;d<32;++d) q[d]=b2f(qkv[s*3072+h*32+d]);
  int lo = s-255<0?0:s-255;
  float m=-1e30f;
  for(int j=lo;j<=s;++j){ float a=0;
    #pragma unroll
    for(int d=0;d<32;++d) a+=q[d]*b2f(qkv[j*3072+1024+h*32+d]); a=rb(rb(a)*0.17677669529663687f); m=fmaxf(m,a);}
  float l=0;
  for(int j=lo;j<=s;++j){ float a=0;
    #pragma unroll
    for(int d=0;d<32;++d) a+=q[d]*b2f(qkv[j*3072+1024+h*32+d]); a=rb(rb(a)*0.17677669529663687f); l+=__expf(a-m);}
  float acc[32];
  #pragma unroll
  for(int d=0;d<32;++d) acc[d]=0;
  for(int j=lo;j<=s;++j){ float a=0;
    #pragma unroll
    for(int d=0;d<32;++d) a+=q[d]*b2f(qkv[j*3072+1024+h*32+d]); a=rb(rb(a)*0.17677669529663687f); float p=rb(__expf(a-m)/l);
    #pragma unroll
    for(int d=0;d<32;++d) acc[d]+=p*b2f(qkv[j*3072+2048+h*32+d]); }
  #pragma unroll
  for(int d=0;d<32;++d) o[s*1024+h*32+d]=f2b(acc[d]);
}
extern "C" void kernel_launch(void* const* d_in, const int* in_sizes, int n_in, void* d_out, int out_size, void* d_ws, size_t ws_size, hipStream_t stream){
  const u16* q=(const u16*)d_in[0]; const u16* k=(const u16*)d_in[1]; const u16* v=(const u16*)d_in[2];
  const u16* sk=(const u16*)d_in[3]; const u16* h=(const u16*)d_in[4]; const u16* Dv=(const u16*)d_in[5];
  const u16* x=(const u16*)d_in[6]; const u16* wqkv=(const u16*)d_in[7]; const u16* wo=(const u16*)d_in[8];
  u16* out=(u16*)d_out; u16* ws=(u16*)d_ws;
  u16* kv=ws; u16* qkv=ws+S*D; u16* o=qkv+S*3072;
  k_kv<<<S*D/256,256,0,stream>>>(k,v,sk,kv);
  k_conv<<<S*D/256,256,0,stream>>>(kv,h,Dv,q,out);
  k_gemm<<<dim3(3072/256,S),256,0,stream>>>(x,wqkv,qkv,3072,3072);
  k_attn<<<S*32/256,256,0,stream>>>(qkv,o);
  k_gemm<<<dim3(1024/256,S),256,0,stream>>>(o,wo,out+1024,1024,2048);
}
```

```cpp
#include <hip/hip_runtime.h>
#include <hip/hip_bf16.h>

#define ATT0_ASM \
  "ds_read_b128 v[132:135], %[ka] offset:32768\n\t" \
  "ds_read_b128 v[136:139], %[ka] offset:33280\n\t" \
  "ds_read_b128 v[140:143], %[ka] offset:0\n\t" \
  "ds_read_b128 v[144:147], %[ka] offset:512\n\t" \
  "s_waitcnt lgkmcnt(3)\n\t" \
  "v_mfma_f32_32x32x16_bf16 v[80:95], v[132:135], %[q0], 0\n\t" \
  "s_waitcnt lgkmcnt(2)\n\t" \
  "v_mfma_f32_32x32x16_bf16 v[80:95], v[136:139], %[q1], v[80:95]\n\t" \
  "s_waitcnt lgkmcnt(1)\n\t" \
  "s_cmp_gt_i32 %[i0], 0\n\t" \
  "s_cbranch_scc1 40f\n\t" \
  "v_mfma_f32_32x32x16_bf16 v[32:47], v[140:143], %[q0], 0\n\t" \
  "41:\n\t" \
  "s_waitcnt lgkmcnt(0)\n\t" \
  "v_mfma_f32_32x32x16_bf16 v[32:47], v[144:147], %[q1], v[32:47]\n\t" \
  "s_mov_b32 vcc_lo, 0xffffffff\n\t" \
  "s_mov_b32 vcc_hi, 0xfffffff0\n\t" \
  "s_nop 9\n\t" \
  "v_cndmask_b32 v80, v32, v80, vcc\n\t" \
  "s_mov_b32 vcc_lo, 0xfffffffe\n\t" \
  "s_mov_b32 vcc_hi, 0xffffffe0\n\t" \
  "v_cndmask_b32 v81, v33, v81, vcc\n\t" \
  "s_mov_b32 vcc_lo, 0xfffffffc\n\t" \
  "s_mov_b32 vcc_hi, 0xffffffc0\n\t" \
  "v_cndmask_b32 v82, v34, v82, vcc\n\t" \
  "s_mov_b32 vcc_lo, 0xfffffff8\n\t" \
  "s_mov_b32 vcc_hi, 0xffffff80\n\t" \
  "v_cndmask_b32 v83, v35, v83, vcc\n\t" \
  "s_mov_b32 vcc_lo, 0xffff0000\n\t" \
  "s_mov_b32 vcc_hi, 0xfff00000\n\t" \
  "v_cndmask_b32 v84, v36, v84, vcc\n\t" \
  "s_mov_b32 vcc_lo, 0xfffe0000\n\t" \
  "s_mov_b32 vcc_hi, 0xffe00000\n\t" \
  "v_cndmask_b32 v85, v37, v85, vcc\n\t" \
  "s_mov_b32 vcc_lo, 0xfffc0000\n\t" \
  "s_mov_b32 vcc_hi, 0xffc00000\n\t" \
  "v_cndmask_b32 v86, v38, v86, vcc\n\t" \
  "s_mov_b32 vcc_lo, 0xfff80000\n\t" \
  "s_mov_b32 vcc_hi, 0xff800000\n\t" \
  "v_cndmask_b32 v87, v39, v87, vcc\n\t" \
  "s_mov_b32 vcc_lo, 0xffffff00\n\t" \
  "s_mov_b32 vcc_hi, 0xfffff000\n\t" \
  "v_cndmask_b32 v88, v40, v88, vcc\n\t" \
  "s_mov_b32 vcc_lo, 0xfffffe00\n\t" \
  "s_mov_b32 vcc_hi, 0xffffe000\n\t" \
  "v_cndmask_b32 v89, v41, v89, vcc\n\t" \
  "s_mov_b32 vcc_lo, 0xfffffc00\n\t" \
  "s_mov_b32 vcc_hi, 0xffffc000\n\t" \
  "v_cndmask_b32 v90, v42, v90, vcc\n\t" \
  "s_mov_b32 vcc_lo, 0xfffff800\n\t" \
  "s_mov_b32 vcc_hi, 0xffff8000\n\t" \
  "v_cndmask_b32 v91, v43, v91, vcc\n\t" \
  "s_mov_b32 vcc_lo, 0xff000000\n\t" \
  "s_mov_b32 vcc_hi, 0xf0000000\n\t" \
  "v_cndmask_b32 v92, v44, v92, vcc\n\t" \
  "s_mov_b32 vcc_lo, 0xfe000000\n\t" \
  "s_mov_b32 vcc_hi, 0xe0000000\n\t" \
  "v_cndmask_b32 v93, v45, v93, vcc\n\t" \
  "s_mov_b32 vcc_lo, 0xfc000000\n\t" \
  "s_mov_b32 vcc_hi, 0xc0000000\n\t" \
  "v_cndmask_b32 v94, v46, v94, vcc\n\t" \
  "s_mov_b32 vcc_lo, 0xf8000000\n\t" \
  "s_mov_b32 vcc_hi, 0x80000000\n\t" \
  "v_cndmask_b32 v95, v47, v95, vcc\n\t" \
  "v_max3_f32 v212, v80, v81, v82\n\t" \
  "v_max3_f32 v213, v83, v84, v85\n\t" \
  "v_max3_f32 v212, v212, v86, v87\n\t" \
  "v_max3_f32 v213, v213, v88, v89\n\t" \
  "v_max3_f32 v212, v212, v90, v91\n\t" \
  "v_max3_f32 v213, v213, v92, v93\n\t" \
  "v_max3_f32 v212, v212, v94, v95\n\t" \
  "v_max_f32 v212, v212, v213\n\t" \
  "v_mov_b32 v213, v212\n\t" \
  "s_nop 1\n\t" \
  "v_permlane32_swap_b32 v212, v213\n\t" \
  "s_nop 1\n\t" \
  "v_max_f32 %[mx], v212, v213\n\t" \
  "s_branch 99f\n\t" \
  "40:\n\t" \
  "s_nop 7\n\t" \
  "v_mov_b32 v32, 0xff800000\n\t" \
  "v_mov_b32 v33, 0xff800000\n\t" \
  "v_mov_b32 v34, 0xff800000\n\t" \
  "v_mov_b32 v35, 0xff800000\n\t" \
  "v_mov_b32 v36, 0xff800000\n\t" \
  "v_mov_b32 v37, 0xff800000\n\t" \
  "v_mov_b32 v38, 0xff800000\n\t" \
  "v_mov_b32 v39, 0xff800000\n\t" \
  "v_mov_b32 v40, 0xff800000\n\t" \
  "v_mov_b32 v41, 0xff800000\n\t" \
  "v_mov_b32 v42, 0xff800000\n\t" \
  "v_mov_b32 v43, 0xff800000\n\t" \
  "v_mov_b32 v44, 0xff800000\n\t" \
  "v_mov_b32 v45, 0xff800000\n\t" \
  "v_mov_b32 v46, 0xff800000\n\t" \
  "v_mov_b32 v47, 0xff800000\n\t" \
  "s_nop 1\n\t" \
  "s_branch 41b\n\t" \
  "99:\n\t"
#define ATT1_ASM \
  "ds_read_b128 v[132:135], %[ka] offset:32768\n\t" \
  "ds_read_b128 v[136:139], %[ka] offset:33280\n\t" \
  "ds_read_b128 v[140:143], %[ka] offset:0\n\t" \
  "ds_read_b128 v[144:147], %[ka] offset:512\n\t" \
  "ds_read_b128 v[148:151], %[ka] offset:4096\n\t" \
  "ds_read_b128 v[152:155], %[ka] offset:4608\n\t" \
  "ds_read_b128 v[156:159], %[ka] offset:8192\n\t" \
  "ds_read_b128 v[160:163], %[ka] offset:8704\n\t" \
  "s_waitcnt lgkmcnt(7)\n\t" \
  "v_mfma_f32_32x32x16_bf16 v[80:95], v[132:135], %[q0], 0\n\t" \
  "s_waitcnt lgkmcnt(6)\n\t" \
  "v_mfma_f32_32x32x16_bf16 v[80:95], v[136:139], %[q1], v[80:95]\n\t" \
  "ds_read_b128 v[132:135], %[ka] offset:12288\n\t" \
  "ds_read_b128 v[136:139], %[ka] offset:12800\n\t" \
  "s_waitcnt lgkmcnt(7)\n\t" \
  "s_cmp_gt_i32 %[i0], 0\n\t" \
  "s_cbranch_scc1 40f\n\t" \
  "v_mfma_f32_32x32x16_bf16 v[32:47], v[140:143], %[q0], 0\n\t" \
  "41:\n\t" \
  "s_waitcnt lgkmcnt(6)\n\t" \
  "v_mfma_f32_32x32x16_bf16 v[32:47], v[144:147], %[q1], v[32:47]\n\t" \
  "ds_read_b128 v[140:143], %[ka] offset:16384\n\t" \
  "ds_read_b128 v[144:147], %[ka] offset:16896\n\t" \
  "s_waitcnt lgkmcnt(7)\n\t" \
  "s_cmp_gt_i32 %[i0], 1\n\t" \
  "s_cbranch_scc1 42f\n\t" \
  "v_mfma_f32_32x32x16_bf16 v[48:63], v[148:151], %[q0], 0\n\t" \
  "43:\n\t" \
  "s_waitcnt lgkmcnt(6)\n\t" \
  "v_mfma_f32_32x32x16_bf16 v[48:63], v[152:155], %[q1], v[48:63]\n\t" \
  "ds_read_b128 v[148:151], %[ka] offset:20480\n\t" \
  "ds_read_b128 v[152:155], %[ka] offset:20992\n\t" \
  "s_mov_b32 vcc_lo, 0xffffffff\n\t" \
  "s_mov_b32 vcc_hi, 0xfffffff0\n\t" \
  "v_cndmask_b32 v80, v32, v80, vcc\n\t" \
  "s_mov_b32 vcc_lo, 0xfffffffe\n\t" \
  "s_mov_b32 vcc_hi, 0xffffffe0\n\t" \
  "v_cndmask_b32 v81, v33, v81, vcc\n\t" \
  "s_mov_b32 vcc_lo, 0xfffffffc\n\t" \
  "s_mov_b32 vcc_hi, 0xffffffc0\n\t" \
  "v_cndmask_b32 v82, v34, v82, vcc\n\t" \
  "s_mov_b32 vcc_lo, 0xfffffff8\n\t" \
  "s_mov_b32 vcc_hi, 0xffffff80\n\t" \
  "v_cndmask_b32 v83, v35, v83, vcc\n\t" \
  "s_mov_b32 vcc_lo, 0xffff0000\n\t" \
  "s_mov_b32 vcc_hi, 0xfff00000\n\t" \
  "v_cndmask_b32 v84, v36, v84, vcc\n\t" \
  "s_mov_b32 vcc_lo, 0xfffe0000\n\t" \
  "s_mov_b32 vcc_hi, 0xffe00000\n\t" \
  "v_cndmask_b32 v85, v37, v85, vcc\n\t" \
  "s_mov_b32 vcc_lo, 0xfffc0000\n\t" \
  "s_mov_b32 vcc_hi, 0xffc00000\n\t" \
  "v_cndmask_b32 v86, v38, v86, vcc\n\t" \
  "s_mov_b32 vcc_lo, 0xfff80000\n\t" \
  "s_mov_b32 vcc_hi, 0xff800000\n\t" \
  "v_cndmask_b32 v87, v39, v87, vcc\n\t" \
  "s_waitcnt lgkmcnt(7)\n\t" \
  "s_cmp_gt_i32 %[i0], 2\n\t" \
  "s_cbranch_scc1 44f\n\t" \
  "v_mfma_f32_32x32x16_bf16 v[64:79], v[156:159], %[q0], 0\n\t" \
  "45:\n\t" \
  "s_mov_b32 vcc_lo, 0xffffff00\n\t" \
  "s_mov_b32 vcc_hi, 0xfffff000\n\t" \
  "v_cndmask_b32 v88, v40, v88, vcc\n\t" \
  "s_mov_b32 vcc_lo, 0xfffffe00\n\t" \
  "s_mov_b32 vcc_hi, 0xffffe000\n\t" \
  "v_cndmask_b32 v89, v41, v89, vcc\n\t" \
  "s_mov_b32 vcc_lo, 0xfffffc00\n\t" \
  "s_mov_b32 vcc_hi, 0xffffc000\n\t" \
  "v_cndmask_b32 v90, v42, v90, vcc\n\t" \
  "s_mov_b32 vcc_lo, 0xfffff800\n\t" \
  "s_mov_b32 vcc_hi, 0xffff8000\n\t" \
  "v_cndmask_b32 v91, v43, v91, vcc\n\t" \
  "s_mov_b32 vcc_lo, 0xff000000\n\t" \
  "s_mov_b32 vcc_hi, 0xf0000000\n\t" \
  "v_cndmask_b32 v92, v44, v92, vcc\n\t" \
  "s_mov_b32 vcc_lo, 0xfe000000\n\t" \
  "s_mov_b32 vcc_hi, 0xe0000000\n\t" \
  "v_cndmask_b32 v93, v45, v93, vcc\n\t" \
  "s_mov_b32 vcc_lo, 0xfc000000\n\t" \
  "s_mov_b32 vcc_hi, 0xc0000000\n\t" \
  "v_cndmask_b32 v94, v46, v94, vcc\n\t" \
  "s_mov_b32 vcc_lo, 0xf8000000\n\t" \
  "s_mov_b32 vcc_hi, 0x80000000\n\t" \
  "v_cndmask_b32 v95, v47, v95, vcc\n\t" \
  "s_waitcnt lgkmcnt(6)\n\t" \
  "v_mfma_f32_32x32x16_bf16 v[64:79], v[160:163], %[q1], v[64:79]\n\t" \
  "ds_read_b128 v[156:159], %[ka] offset:24576\n\t" \
  "ds_read_b128 v[160:163], %[ka] offset:25088\n\t" \
  "v_max3_f32 v212, v48, v49, v50\n\t" \
  "v_max3_f32 v213, v51, v52, v53\n\t" \
  "v_max3_f32 v212, v212, v54, v55\n\t" \
  "s_waitcnt lgkmcnt(7)\n\t" \
  "s_cmp_gt_i32 %[i0], 3\n\t" \
  "s_cbranch_scc1 46f\n\t" \
  "v_mfma_f32_32x32x16_bf16 v[32:47], v[132:135], %[q0], 0\n\t" \
  "47:\n\t" \
  "v_max3_f32 v213, v213, v56, v57\n\t" \
  "v_max3_f32 v212, v212, v58, v59\n\t" \
  "v_max3_f32 v213, v213, v60, v61\n\t" \
  "v_max3_f32 v212, v212, v62, v63\n\t" \
  "s_waitcnt lgkmcnt(6)\n\t" \
  "v_mfma_f32_32x32x16_bf16 v[32:47], v[136:139], %[q1], v[32:47]\n\t" \
  "ds_read_b128 v[132:135], %[ka] offset:28672\n\t" \
  "ds_read_b128 v[136:139], %[ka] offset:29184\n\t" \
  "v_max3_f32 v212, v212, v80, v81\n\t" \
  "v_max3_f32 v213, v213, v82, v83\n\t" \
  "v_max3_f32 v212, v212, v84, v85\n\t" \
  "v_max3_f32 v213, v213, v86, v87\n\t" \
  "v_max3_f32 v212, v212, v88, v89\n\t" \
  "v_max3_f32 v213, v213, v90, v91\n\t" \
  "v_max3_f32 v212, v212, v92, v93\n\t" \
  "v_max3_f32 v213, v213, v94, v95\n\t" \
  "s_waitcnt lgkmcnt(7)\n\t" \
  "s_cmp_gt_i32 %[i0], 4\n\t" \
  "s_cbranch_scc1 48f\n\t" \
  "v_mfma_f32_32x32x16_bf16 v[48:63], v[140:143], %[q0], 0\n\t" \
  "49:\n\t" \
  "v_max3_f32 v212, v212, v64, v65\n\t" \
  "v_max3_f32 v213, v213, v66, v67\n\t" \
  "v_max3_f32 v212, v212, v68, v69\n\t" \
  "v_max3_f32 v213, v213, v70, v71\n\t" \
  "v_max3_f32 v212, v212, v72, v73\n\t" \
  "v_max3_f32 v213, v213, v74, v75\n\t" \
  "v_max3_f32 v212, v212, v76, v77\n\t" \
  "v_max3_f32 v213, v213, v78, v79\n\t" \
  "s_waitcnt lgkmcnt(6)\n\t" \
  "v_mfma_f32_32x32x16_bf16 v[48:63], v[144:147], %[q1], v[48:63]\n\t" \
  "v_max3_f32 v212, v212, v32, v33\n\t" \
  "v_max3_f32 v213, v213, v34, v35\n\t" \
  "v_max3_f32 v212, v212, v36, v37\n\t" \
  "v_max3_f32 v213, v213, v38, v39\n\t" \
  "s_waitcnt lgkmcnt(5)\n\t" \
  "s_cmp_gt_i32 %[i0], 5\n\t" \
  "s_cbranch_scc1 50f\n\t" \
  "v_mfma_f32_32x32x16_bf16 v[64:79], v[148:151], %[q0], 0\n\t" \
  "51:\n\t" \
  "v_max3_f32 v212, v212, v40, v41\n\t" \
  "v_max3_f32 v213, v213, v42, v43\n\t" \
  "v_max3_f32 v212, v212, v44, v45\n\t" \
  "v_max3_f32 v213, v213, v46, v47\n\t" \
  "s_waitcnt lgkmcnt(4)\n\t" \
  "v_mfma_f32_32x32x16_bf16 v[64:79], v[152:155], %[q1], v[64:79]\n\t" \
  "v_max3_f32 v212, v212, v48, v49\n\t" \
  "v_max3_f32 v213, v213, v50, v51\n\t" \
  "v_max3_f32 v212, v212, v52, v53\n\t" \
  "v_max3_f32 v213, v213, v54, v55\n\t" \
  "s_waitcnt lgkmcnt(3)\n\t" \
  "s_cmp_gt_i32 %[i0], 6\n\t" \
  "s_cbranch_scc1 52f\n\t" \
  "v_mfma_f32_32x32x16_bf16 v[32:47], v[156:159], %[q0], 0\n\t" \
  "53:\n\t" \
  "v_max3_f32 v212, v212, v56, v57\n\t" \
  "v_max3_f32 v213, v213, v58, v59\n\t" \
  "v_max3_f32 v212, v212, v60, v61\n\t" \
  "v_max3_f32 v213, v213, v62, v63\n\t" \
  "s_waitcnt lgkmcnt(2)\n\t" \
  "v_mfma_f32_32x32x16_bf16 v[32:47], v[160:163], %[q1], v[32:47]\n\t" \
  "v_max3_f32 v212, v212, v64, v65\n\t" \
  "v_max3_f32 v213, v213, v66, v67\n\t" \
  "v_max3_f32 v212, v212, v68, v69\n\t" \
  "v_max3_f32 v213, v213, v70, v71\n\t" \
  "s_waitcnt lgkmcnt(1)\n\t" \
  "s_cmp_gt_i32 %[i0], 7\n\t" \
  "s_cbranch_scc1 54f\n\t" \
  "v_mfma_f32_32x32x16_bf16 v[48:63], v[132:135], %[q0], 0\n\t" \
  "55:\n\t" \
  "v_max3_f32 v212, v212, v72, v73\n\t" \
  "v_max3_f32 v213, v213, v74, v75\n\t" \
  "v_max3_f32 v212, v212, v76, v77\n\t" \
  "v_max3_f32 v213, v213, v78, v79\n\t" \
  "s_waitcnt lgkmcnt(0)\n\t" \
  "v_mfma_f32_32x32x16_bf16 v[48:63], v[136:139], %[q1], v[48:63]\n\t" \
  "v_max3_f32 v212, v212, v32, v33\n\t" \
  "v_max3_f32 v213, v213, v34, v35\n\t" \
  "v_max3_f32 v212, v212, v36, v37\n\t" \
  "v_max3_f32 v213, v213, v38, v39\n\t" \
  "v_max3_f32 v212, v212, v40, v41\n\t" \
  "v_max3_f32 v213, v213, v42, v43\n\t" \
  "v_max3_f32 v212, v212, v44, v45\n\t" \
  "v_max3_f32 v213, v213, v46, v47\n\t" \
  "s_nop 3\n\t" \
  "v_max3_f32 v212, v212, v48, v49\n\t" \
  "v_max3_f32 v213, v213, v50, v51\n\t" \
  "v_max3_f32 v212, v212, v52, v53\n\t" \
  "v_max3_f32 v213, v213, v54, v55\n\t" \
  "v_max3_f32 v212, v212, v56, v57\n\t" \
  "v_max3_f32 v213, v213, v58, v59\n\t" \
  "v_max3_f32 v212, v212, v60, v61\n\t" \
  "v_max3_f32 v213, v213, v62, v63\n\t" \
  "v_max_f32 v212, v212, v213\n\t" \
  "v_mov_b32 v213, v212\n\t" \
  "s_nop 1\n\t" \
  "v_permlane32_swap_b32 v212, v213\n\t" \
  "s_nop 1\n\t" \
  "v_max_f32 %[mx], v212, v213\n\t" \
  "s_branch 99f\n\t" \
  "40:\n\t" \
  "s_nop 7\n\t" \
  "v_mov_b32 v32, 0xff800000\n\t" \
  "v_mov_b32 v33, 0xff800000\n\t" \
  "v_mov_b32 v34, 0xff800000\n\t" \
  "v_mov_b32 v35, 0xff800000\n\t" \
  "v_mov_b32 v36, 0xff800000\n\t" \
  "v_mov_b32 v37, 0xff800000\n\t" \
  "v_mov_b32 v38, 0xff800000\n\t" \
  "v_mov_b32 v39, 0xff800000\n\t" \
  "v_mov_b32 v40, 0xff800000\n\t" \
  "v_mov_b32 v41, 0xff800000\n\t" \
  "v_mov_b32 v42, 0xff800000\n\t" \
  "v_mov_b32 v43, 0xff800000\n\t" \
  "v_mov_b32 v44, 0xff800000\n\t" \
  "v_mov_b32 v45, 0xff800000\n\t" \
  "v_mov_b32 v46, 0xff800000\n\t" \
  "v_mov_b32 v47, 0xff800000\n\t" \
  "s_nop 1\n\t" \
  "s_branch 41b\n\t" \
  "42:\n\t" \
  "s_nop 7\n\t" \
  "v_mov_b32 v48, 0xff800000\n\t" \
  "v_mov_b32 v49, 0xff800000\n\t" \
  "v_mov_b32 v50, 0xff800000\n\t" \
  "v_mov_b32 v51, 0xff800000\n\t" \
  "v_mov_b32 v52, 0xff800000\n\t" \
  "v_mov_b32 v53, 0xff800000\n\t" \
  "v_mov_b32 v54, 0xff800000\n\t" \
  "v_mov_b32 v55, 0xff800000\n\t" \
  "v_mov_b32 v56, 0xff800000\n\t" \
  "v_mov_b32 v57, 0xff800000\n\t" \
  "v_mov_b32 v58, 0xff800000\n\t" \
  "v_mov_b32 v59, 0xff800000\n\t" \
  "v_mov_b32 v60, 0xff800000\n\t" \
  "v_mov_b32 v61, 0xff800000\n\t" \
  "v_mov_b32 v62, 0xff800000\n\t" \
  "v_mov_b32 v63, 0xff800000\n\t" \
  "s_nop 1\n\t" \
  "s_branch 43b\n\t" \
  "44:\n\t" \
  "s_nop 7\n\t" \
  "v_mov_b32 v64, 0xff800000\n\t" \
  "v_mov_b32 v65, 0xff800000\n\t" \
  "v_mov_b32 v66, 0xff800000\n\t" \
  "v_mov_b32 v67, 0xff800000\n\t" \
  "v_mov_b32 v68, 0xff800000\n\t" \
  "v_mov_b32 v69, 0xff800000\n\t" \
  "v_mov_b32 v70, 0xff800000\n\t" \
  "v_mov_b32 v71, 0xff800000\n\t" \
  "v_mov_b32 v72, 0xff800000\n\t" \
  "v_mov_b32 v73, 0xff800000\n\t" \
  "v_mov_b32 v74, 0xff800000\n\t" \
  "v_mov_b32 v75, 0xff800000\n\t" \
  "v_mov_b32 v76, 0xff800000\n\t" \
  "v_mov_b32 v77, 0xff800000\n\t" \
  "v_mov_b32 v78, 0xff800000\n\t" \
  "v_mov_b32 v79, 0xff800000\n\t" \
  "s_nop 1\n\t" \
  "s_branch 45b\n\t" \
  "46:\n\t" \
  "s_nop 7\n\t" \
  "v_mov_b32 v32, 0xff800000\n\t" \
  "v_mov_b32 v33, 0xff800000\n\t" \
  "v_mov_b32 v34, 0xff800000\n\t" \
  "v_mov_b32 v35, 0xff800000\n\t" \
  "v_mov_b32 v36, 0xff800000\n\t" \
  "v_mov_b32 v37, 0xff800000\n\t" \
  "v_mov_b32 v38, 0xff800000\n\t" \
  "v_mov_b32 v39, 0xff800000\n\t" \
  "v_mov_b32 v40, 0xff800000\n\t" \
  "v_mov_b32 v41, 0xff800000\n\t" \
  "v_mov_b32 v42, 0xff800000\n\t" \
  "v_mov_b32 v43, 0xff800000\n\t" \
  "v_mov_b32 v44, 0xff800000\n\t" \
  "v_mov_b32 v45, 0xff800000\n\t" \
  "v_mov_b32 v46, 0xff800000\n\t" \
  "v_mov_b32 v47, 0xff800000\n\t" \
  "s_nop 1\n\t" \
  "s_branch 47b\n\t" \
  "48:\n\t" \
  "s_nop 7\n\t" \
  "v_mov_b32 v48, 0xff800000\n\t" \
  "v_mov_b32 v49, 0xff800000\n\t" \
  "v_mov_b32 v50, 0xff800000\n\t" \
  "v_mov_b32 v51, 0xff800000\n\t" \
  "v_mov_b32 v52, 0xff800000\n\t" \
  "v_mov_b32 v53, 0xff800000\n\t" \
  "v_mov_b32 v54, 0xff800000\n\t" \
  "v_mov_b32 v55, 0xff800000\n\t" \
  "v_mov_b32 v56, 0xff800000\n\t" \
  "v_mov_b32 v57, 0xff800000\n\t" \
  "v_mov_b32 v58, 0xff800000\n\t" \
  "v_mov_b32 v59, 0xff800000\n\t" \
  "v_mov_b32 v60, 0xff800000\n\t" \
  "v_mov_b32 v61, 0xff800000\n\t" \
  "v_mov_b32 v62, 0xff800000\n\t" \
  "v_mov_b32 v63, 0xff800000\n\t" \
  "s_nop 1\n\t" \
  "s_branch 49b\n\t" \
  "50:\n\t" \
  "s_nop 7\n\t" \
  "v_mov_b32 v64, 0xff800000\n\t" \
  "v_mov_b32 v65, 0xff800000\n\t" \
  "v_mov_b32 v66, 0xff800000\n\t" \
  "v_mov_b32 v67, 0xff800000\n\t" \
  "v_mov_b32 v68, 0xff800000\n\t" \
  "v_mov_b32 v69, 0xff800000\n\t" \
  "v_mov_b32 v70, 0xff800000\n\t" \
  "v_mov_b32 v71, 0xff800000\n\t" \
  "v_mov_b32 v72, 0xff800000\n\t" \
  "v_mov_b32 v73, 0xff800000\n\t" \
  "v_mov_b32 v74, 0xff800000\n\t" \
  "v_mov_b32 v75, 0xff800000\n\t" \
  "v_mov_b32 v76, 0xff800000\n\t" \
  "v_mov_b32 v77, 0xff800000\n\t" \
  "v_mov_b32 v78, 0xff800000\n\t" \
  "v_mov_b32 v79, 0xff800000\n\t" \
  "s_nop 1\n\t" \
  "s_branch 51b\n\t" \
  "52:\n\t" \
  "s_nop 7\n\t" \
  "v_mov_b32 v32, 0xff800000\n\t" \
  "v_mov_b32 v33, 0xff800000\n\t" \
  "v_mov_b32 v34, 0xff800000\n\t" \
  "v_mov_b32 v35, 0xff800000\n\t" \
  "v_mov_b32 v36, 0xff800000\n\t" \
  "v_mov_b32 v37, 0xff800000\n\t" \
  "v_mov_b32 v38, 0xff800000\n\t" \
  "v_mov_b32 v39, 0xff800000\n\t" \
  "v_mov_b32 v40, 0xff800000\n\t" \
  "v_mov_b32 v41, 0xff800000\n\t" \
  "v_mov_b32 v42, 0xff800000\n\t" \
  "v_mov_b32 v43, 0xff800000\n\t" \
  "v_mov_b32 v44, 0xff800000\n\t" \
  "v_mov_b32 v45, 0xff800000\n\t" \
  "v_mov_b32 v46, 0xff800000\n\t" \
  "v_mov_b32 v47, 0xff800000\n\t" \
  "s_nop 1\n\t" \
  "s_branch 53b\n\t" \
  "54:\n\t" \
  "s_nop 7\n\t" \
  "v_mov_b32 v48, 0xff800000\n\t" \
  "v_mov_b32 v49, 0xff800000\n\t" \
  "v_mov_b32 v50, 0xff800000\n\t" \
  "v_mov_b32 v51, 0xff800000\n\t" \
  "v_mov_b32 v52, 0xff800000\n\t" \
  "v_mov_b32 v53, 0xff800000\n\t" \
  "v_mov_b32 v54, 0xff800000\n\t" \
  "v_mov_b32 v55, 0xff800000\n\t" \
  "v_mov_b32 v56, 0xff800000\n\t" \
  "v_mov_b32 v57, 0xff800000\n\t" \
  "v_mov_b32 v58, 0xff800000\n\t" \
  "v_mov_b32 v59, 0xff800000\n\t" \
  "v_mov_b32 v60, 0xff800000\n\t" \
  "v_mov_b32 v61, 0xff800000\n\t" \
  "v_mov_b32 v62, 0xff800000\n\t" \
  "v_mov_b32 v63, 0xff800000\n\t" \
  "s_nop 1\n\t" \
  "s_branch 55b\n\t" \
  "99:\n\t"
#define ATT1_CLOB "v32","v33","v34","v35","v36","v37","v38","v39","v40","v41","v42","v43","v44","v45","v46","v47","v48","v49","v50","v51","v52","v53","v54","v55","v56","v57","v58","v59","v60","v61","v62","v63","v64","v65","v66","v67","v68","v69","v70","v71","v72","v73","v74","v75","v76","v77","v78","v79","v132","v133","v134","v135","v136","v137","v138","v139","v140","v141","v142","v143","v144","v145","v146","v147","v148","v149","v150","v151","v152","v153","v154","v155","v156","v157","v158","v159","v160","v161","v162","v163","v212","v213","vcc","scc"
#define ATT2_ASM \
  "ds_read_b128 v[140:143], %[ka] offset:4096\n\t" \
  "ds_read_b128 v[144:147], %[ka] offset:4608\n\t" \
  "ds_read_b128 v[164:167], %[va] offset:32768\n\t" \
  "ds_read_b128 v[168:171], %[va] offset:33280\n\t" \
  "ds_read_b128 v[148:151], %[ka] offset:8192\n\t" \
  "ds_read_b128 v[152:155], %[ka] offset:8704\n\t" \
  "ds_read_b128 v[172:175], %[va] offset:0\n\t" \
  "ds_read_b128 v[176:179], %[va] offset:512\n\t" \
  "ds_read_b128 v[180:183], %[va] offset:4096\n\t" \
  "ds_read_b128 v[184:187], %[va] offset:4608\n\t" \
  "v_sub_f32 v212, 0, %[mx]\n\t" \
  "v_sub_f32 v96, %[c0], %[mx]\n\t" \
  "v_mov_b32 v97, v96\n\t" \
  "v_mov_b32 v98, v96\n\t" \
  "v_mov_b32 v99, v96\n\t" \
  "v_mov_b32 v100, v96\n\t" \
  "v_mov_b32 v101, v96\n\t" \
  "v_mov_b32 v102, v96\n\t" \
  "v_mov_b32 v103, v96\n\t" \
  "v_mov_b32 v104, v96\n\t" \
  "v_mov_b32 v105, v96\n\t" \
  "v_mov_b32 v106, v96\n\t" \
  "v_mov_b32 v107, v96\n\t" \
  "v_mov_b32 v108, v96\n\t" \
  "v_mov_b32 v109, v96\n\t" \
  "v_mov_b32 v110, v96\n\t" \
  "v_mov_b32 v111, v96\n\t" \
  "v_add_f32 v80, v212, v80\n\t" \
  "v_add_f32 v81, v212, v81\n\t" \
  "v_add_f32 v82, v212, v82\n\t" \
  "v_add_f32 v83, v212, v83\n\t" \
  "v_add_f32 v84, v212, v84\n\t" \
  "v_add_f32 v85, v212, v85\n\t" \
  "v_add_f32 v86, v212, v86\n\t" \
  "v_add_f32 v87, v212, v87\n\t" \
  "s_waitcnt lgkmcnt(9)\n\t" \
  "v_mfma_f32_32x32x16_bf16 v[48:63], v[140:143], %[q0], v[96:111]\n\t" \
  "v_add_f32 v88, v212, v88\n\t" \
  "v_add_f32 v89, v212, v89\n\t" \
  "v_add_f32 v90, v212, v90\n\t" \
  "v_add_f32 v91, v212, v91\n\t" \
  "v_add_f32 v92, v212, v92\n\t" \
  "v_add_f32 v93, v212, v93\n\t" \
  "v_add_f32 v94, v212, v94\n\t" \
  "v_add_f32 v95, v212, v95\n\t" \
  "s_waitcnt lgkmcnt(8)\n\t" \
  "v_mfma_f32_32x32x16_bf16 v[48:63], v[144:147], %[q1], v[48:63]\n\t" \
  "v_exp_f32 v80, v80\n\t" \
  "v_exp_f32 v81, v81\n\t" \
  "v_exp_f32 v82, v82\n\t" \
  "v_exp_f32 v83, v83\n\t" \
  "v_exp_f32 v84, v84\n\t" \
  "v_exp_f32 v85, v85\n\t" \
  "v_exp_f32 v86, v86\n\t" \
  "v_exp_f32 v87, v87\n\t" \
  "v_cvt_pk_bf16_f32 v196, v80, v81\n\t" \
  "v_cvt_pk_bf16_f32 v197, v82, v83\n\t" \
  "v_cvt_pk_bf16_f32 v198, v84, v85\n\t" \
  "v_cvt_pk_bf16_f32 v199, v86, v87\n\t" \
  "v_and_b32 v64, v244, v196\n\t" \
  "v_and_b32 v65, v245, v197\n\t" \
  "v_and_b32 v66, v246, v198\n\t" \
  "v_and_b32 v67, v247, v199\n\t" \
  "v_mfma_f32_16x16x32_bf16 v[128:131], %[ones], v[196:199], 0\n\t" \
  "v_xor_b32 v204, v64, v196\n\t" \
  "v_xor_b32 v205, v65, v197\n\t" \
  "v_xor_b32 v206, v66, v198\n\t" \
  "v_xor_b32 v207, v67, v199\n\t" \
  "s_waitcnt lgkmcnt(7)\n\t" \
  "v_mfma_f32_32x32x16_bf16 v[112:127], v[164:167], v[64:67], 0\n\t" \
  "v_exp_f32 v88, v88\n\t" \
  "v_exp_f32 v89, v89\n\t" \
  "v_exp_f32 v90, v90\n\t" \
  "v_exp_f32 v91, v91\n\t" \
  "v_exp_f32 v92, v92\n\t" \
  "v_exp_f32 v93, v93\n\t" \
  "v_exp_f32 v94, v94\n\t" \
  "v_exp_f32 v95, v95\n\t" \
  "v_cvt_pk_bf16_f32 v200, v88, v89\n\t" \
  "v_cvt_pk_bf16_f32 v201, v90, v91\n\t" \
  "v_cvt_pk_bf16_f32 v202, v92, v93\n\t" \
  "v_cvt_pk_bf16_f32 v203, v94, v95\n\t" \
  "v_and_b32 v68, v248, v200\n\t" \
  "v_and_b32 v69, v249, v201\n\t" \
  "v_and_b32 v70, v250, v202\n\t" \
  "v_and_b32 v71, v251, v203\n\t" \
  "v_mfma_f32_16x16x32_bf16 v[128:131], %[ones], v[200:203], v[128:131]\n\t" \
  "v_xor_b32 v208, v68, v200\n\t" \
  "v_xor_b32 v209, v69, v201\n\t" \
  "v_xor_b32 v210, v70, v202\n\t" \
  "v_xor_b32 v211, v71, v203\n\t" \
  "s_waitcnt lgkmcnt(6)\n\t" \
  "v_mfma_f32_32x32x16_bf16 v[112:127], v[168:171], v[68:71], v[112:127]\n\t" \
  "v_exp_f32 v48, v48\n\t" \
  "v_exp_f32 v49, v49\n\t" \
  "v_exp_f32 v50, v50\n\t" \
  "s_waitcnt lgkmcnt(3)\n\t" \
  "v_mfma_f32_32x32x16_bf16 v[112:127], v[172:175], v[204:207], v[112:127]\n\t" \
  "v_exp_f32 v51, v51\n\t" \
  "v_exp_f32 v52, v52\n\t" \
  "v_exp_f32 v53, v53\n\t" \
  "v_exp_f32 v54, v54\n\t" \
  "s_cmp_eq_i32 %[i0], 2\n\t" \
  "s_cbranch_scc1 40f\n\t" \
  "41:\n\t" \
  "v_mfma_f32_32x32x16_bf16 v[32:47], v[148:151], %[q0], v[96:111]\n\t" \
  "v_exp_f32 v55, v55\n\t" \
  "v_cvt_pk_bf16_f32 v196, v48, v49\n\t" \
  "v_cvt_pk_bf16_f32 v197, v50, v51\n\t" \
  "s_waitcnt lgkmcnt(2)\n\t" \
  "v_mfma_f32_32x32x16_bf16 v[112:127], v[176:179], v[208:211], v[112:127]\n\t" \
  "v_cvt_pk_bf16_f32 v198, v52, v53\n\t" \
  "v_cvt_pk_bf16_f32 v199, v54, v55\n\t" \
  "v_mfma_f32_32x32x16_bf16 v[32:47], v[152:155], %[q1], v[32:47]\n\t" \
  "v_exp_f32 v56, v56\n\t" \
  "ds_read_b128 v[156:159], %[ka] offset:12288\n\t" \
  "v_exp_f32 v57, v57\n\t" \
  "ds_read_b128 v[160:163], %[ka] offset:12800\n\t" \
  "v_exp_f32 v58, v58\n\t" \
  "v_exp_f32 v59, v59\n\t" \
  "s_waitcnt lgkmcnt(3)\n\t" \
  "v_mfma_f32_32x32x16_bf16 v[112:127], v[180:183], v[196:199], v[112:127]\n\t" \
  "v_exp_f32 v60, v60\n\t" \
  "ds_read_b128 v[188:191], %[va] offset:8192\n\t" \
  "v_exp_f32 v61, v61\n\t" \
  "ds_read_b128 v[192:195], %[va] offset:8704\n\t" \
  "v_exp_f32 v62, v62\n\t" \
  "v_exp_f32 v63, v63\n\t" \
  "v_mfma_f32_16x16x32_bf16 v[128:131], %[ones], v[196:199], v[128:131]\n\t" \
  "v_cvt_pk_bf16_f32 v200, v56, v57\n\t" \
  "v_cvt_pk_bf16_f32 v201, v58, v59\n\t" \
  "v_cvt_pk_bf16_f32 v202, v60, v61\n\t" \
  "s_cmp_lg_u32 %[par], 0\n\t" \
  "s_cbranch_scc1 95f\n\t" \
  "s_waitcnt vmcnt(1)\n\t" \
  "s_mov_b64 exec, 1\n\t" \
  "global_store_dwordx2 %[fo], %[tg], %[wsb] sc1\n\t" \
  "s_mov_b64 exec, -1\n\t" \
  "global_load_dwordx4 v[80:83], %[qo], %[qb]\n\t" \
  "global_load_dwordx4 v[84:87], %[qo], %[qb] offset:2048\n\t" \
  "global_load_dwordx4 v[88:91], %[qo], %[qb2]\n\t" \
  "global_load_dwordx4 v[92:95], %[qo], %[qb2] offset:2048\n\t" \
  "95:\n\t" \
  "v_cvt_pk_bf16_f32 v203, v62, v63\n\t" \
  "v_exp_f32 v32, v32\n\t" \
  "v_exp_f32 v33, v33\n\t" \
  "s_waitcnt lgkmcnt(4)\n\t" \
  "v_mfma_f32_32x32x16_bf16 v[112:127], v[184:187], v[200:203], v[112:127]\n\t" \
  "v_exp_f32 v34, v34\n\t" \
  "v_exp_f32 v35, v35\n\t" \
  "v_mfma_f32_16x16x32_bf16 v[128:131], %[ones], v[200:203], v[128:131]\n\t" \
  "v_exp_f32 v36, v36\n\t" \
  "v_exp_f32 v37, v37\n\t" \
  "s_mov_b32 m0, %[wm]\n\t" \
  "s_add_u32 %[wm], %[wm], 0x400\n\t" \
  "global_load_lds_dwordx4 %[wv], %[wb0]\n\t" \
  "v_add_u32 %[wv], 0x4000, %[wv]\n\t" \
  "v_exp_f32 v38, v38\n\t" \
  "s_waitcnt lgkmcnt(3)\n\t" \
  "s_cmp_eq_i32 %[i0], 3\n\t" \
  "s_cbranch_scc1 42f\n\t" \
  "43:\n\t" \
  "v_mfma_f32_32x32x16_bf16 v[48:63], v[156:159], %[q0], v[96:111]\n\t" \
  "v_exp_f32 v39, v39\n\t" \
  "v_cvt_pk_bf16_f32 v196, v32, v33\n\t" \
  "v_cvt_pk_bf16_f32 v197, v34, v35\n\t" \
  "v_cvt_pk_bf16_f32 v198, v36, v37\n\t" \
  "v_cvt_pk_bf16_f32 v199, v38, v39\n\t" \
  "s_waitcnt lgkmcnt(2)\n\t" \
  "v_mfma_f32_32x32x16_bf16 v[48:63], v[160:163], %[q1], v[48:63]\n\t" \
  "v_exp_f32 v40, v40\n\t" \
  "ds_read_b128 v[132:135], %[ka] offset:16384\n\t" \
  "v_exp_f32 v41, v41\n\t" \
  "ds_read_b128 v[136:139], %[ka] offset:16896\n\t" \
  "v_exp_f32 v42, v42\n\t" \
  "v_exp_f32 v43, v43\n\t" \
  "s_waitcnt lgkmcnt(3)\n\t" \
  "v_mfma_f32_32x32x16_bf16 v[112:127], v[188:191], v[196:199], v[112:127]\n\t" \
  "v_exp_f32 v44, v44\n\t" \
  "ds_read_b128 v[164:167], %[va] offset:12288\n\t" \
  "v_exp_f32 v45, v45\n\t" \
  "ds_read_b128 v[168:171], %[va] offset:12800\n\t" \
  "v_exp_f32 v46, v46\n\t" \
  "v_exp_f32 v47, v47\n\t" \
  "v_mfma_f32_16x16x32_bf16 v[128:131], %[ones], v[196:199], v[128:131]\n\t" \
  "v_cvt_pk_bf16_f32 v200, v40, v41\n\t" \
  "v_cvt_pk_bf16_f32 v201, v42, v43\n\t" \
  "v_cvt_pk_bf16_f32 v202, v44, v45\n\t" \
  "s_mov_b32 m0, %[wm]\n\t" \
  "s_add_u32 %[wm], %[wm], 0x400\n\t" \
  "global_load_lds_dwordx4 %[wv], %[wb0]\n\t" \
  "v_add_u32 %[wv], 0x4000, %[wv]\n\t" \
  "v_cvt_pk_bf16_f32 v203, v46, v47\n\t" \
  "v_exp_f32 v48, v48\n\t" \
  "v_exp_f32 v49, v49\n\t" \
  "s_waitcnt lgkmcnt(4)\n\t" \
  "v_mfma_f32_32x32x16_bf16 v[112:127], v[192:195], v[200:203], v[112:127]\n\t" \
  "v_exp_f32 v50, v50\n\t" \
  "v_exp_f32 v51, v51\n\t" \
  "v_mfma_f32_16x16x32_bf16 v[128:131], %[ones], v[200:203], v[128:131]\n\t" \
  "v_exp_f32 v52, v52\n\t" \
  "v_exp_f32 v53, v53\n\t" \
  "s_mov_b32 m0, %[wm]\n\t" \
  "s_add_u32 %[wm], %[wm], 0x400\n\t" \
  "global_load_lds_dwordx4 %[wv], %[wb0]\n\t" \
  "v_add_u32 %[wv], 0x4000, %[wv]\n\t" \
  "v_exp_f32 v54, v54\n\t" \
  "s_waitcnt lgkmcnt(3)\n\t" \
  "s_cmp_eq_i32 %[i0], 4\n\t" \
  "s_cbranch_scc1 44f\n\t" \
  "45:\n\t" \
  "v_mfma_f32_32x32x16_bf16 v[32:47], v[132:135], %[q0], v[96:111]\n\t" \
  "v_exp_f32 v55, v55\n\t" \
  "v_cvt_pk_bf16_f32 v196, v48, v49\n\t" \
  "v_cvt_pk_bf16_f32 v197, v50, v51\n\t" \
  "v_cvt_pk_bf16_f32 v198, v52, v53\n\t" \
  "v_cvt_pk_bf16_f32 v199, v54, v55\n\t" \
  "s_waitcnt lgkmcnt(2)\n\t" \
  "v_mfma_f32_32x32x16_bf16 v[32:47], v[136:139], %[q1], v[32:47]\n\t" \
  "v_exp_f32 v56, v56\n\t" \
  "ds_read_b128 v[140:143], %[ka] offset:20480\n\t" \
  "v_exp_f32 v57, v57\n\t" \
  "ds_read_b128 v[144:147], %[ka] offset:20992\n\t" \
  "v_exp_f32 v58, v58\n\t" \
  "v_exp_f32 v59, v59\n\t" \
  "s_waitcnt lgkmcnt(3)\n\t" \
  "v_mfma_f32_32x32x16_bf16 v[112:127], v[164:167], v[196:199], v[112:127]\n\t" \
  "v_exp_f32 v60, v60\n\t" \
  "ds_read_b128 v[172:175], %[va] offset:16384\n\t" \
  "v_exp_f32 v61, v61\n\t" \
  "ds_read_b128 v[176:179], %[va] offset:16896\n\t" \
  "v_exp_f32 v62, v62\n\t" \
  "v_exp_f32 v63, v63\n\t" \
  "v_mfma_f32_16x16x32_bf16 v[128:131], %[ones], v[196:199], v[128:131]\n\t" \
  "v_cvt_pk_bf16_f32 v200, v56, v57\n\t" \
  "v_cvt_pk_bf16_f32 v201, v58, v59\n\t" \
  "v_cvt_pk_bf16_f32 v202, v60, v61\n\t" \
  "s_mov_b32 m0, %[wm]\n\t" \
  "s_add_u32 %[wm], %[wm], 0x400\n\t" \
  "global_load_lds_dwordx4 %[wv], %[wb0]\n\t" \
  "v_subrev_u32 %[wv], 0xc000, %[wv]\n\t" \
  "v_cvt_pk_bf16_f32 v203, v62, v63\n\t" \
  "v_exp_f32 v32, v32\n\t" \
  "v_exp_f32 v33, v33\n\t" \
  "s_waitcnt lgkmcnt(4)\n\t" \
  "v_mfma_f32_32x32x16_bf16 v[112:127], v[168:171], v[200:203], v[112:127]\n\t" \
  "v_exp_f32 v34, v34\n\t" \
  "v_exp_f32 v35, v35\n\t" \
  "v_mfma_f32_16x16x32_bf16 v[128:131], %[ones], v[200:203], v[128:131]\n\t" \
  "v_exp_f32 v36, v36\n\t" \
  "v_exp_f32 v37, v37\n\t" \
  "s_mov_b32 m0, %[wm]\n\t" \
  "s_add_u32 %[wm], %[wm], 0x400\n\t" \
  "global_load_lds_dwordx4 %[wv], %[wb1]\n\t" \
  "v_add_u32 %[wv], 0x4000, %[wv]\n\t" \
  "v_exp_f32 v38, v38\n\t" \
  "s_waitcnt lgkmcnt(3)\n\t" \
  "s_cmp_eq_i32 %[i0], 5\n\t" \
  "s_cbranch_scc1 46f\n\t" \
  "47:\n\t" \
  "v_mfma_f32_32x32x16_bf16 v[48:63], v[140:143], %[q0], v[96:111]\n\t" \
  "v_exp_f32 v39, v39\n\t" \
  "v_cvt_pk_bf16_f32 v196, v32, v33\n\t" \
  "v_cvt_pk_bf16_f32 v197, v34, v35\n\t" \
  "v_cvt_pk_bf16_f32 v198, v36, v37\n\t" \
  "v_cvt_pk_bf16_f32 v199, v38, v39\n\t" \
  "s_waitcnt lgkmcnt(2)\n\t" \
  "v_mfma_f32_32x32x16_bf16 v[48:63], v[144:147], %[q1], v[48:63]\n\t" \
  "v_exp_f32 v40, v40\n\t" \
  "ds_read_b128 v[148:151], %[ka] offset:24576\n\t" \
  "v_exp_f32 v41, v41\n\t" \
  "ds_read_b128 v[152:155], %[ka] offset:25088\n\t" \
  "v_exp_f32 v42, v42\n\t" \
  "v_exp_f32 v43, v43\n\t" \
  "s_waitcnt lgkmcnt(3)\n\t" \
  "v_mfma_f32_32x32x16_bf16 v[112:127], v[172:175], v[196:199], v[112:127]\n\t" \
  "v_exp_f32 v44, v44\n\t" \
  "ds_read_b128 v[180:183], %[va] offset:20480\n\t" \
  "v_exp_f32 v45, v45\n\t" \
  "ds_read_b128 v[184:187], %[va] offset:20992\n\t" \
  "v_exp_f32 v46, v46\n\t" \
  "v_exp_f32 v47, v47\n\t" \
  "v_mfma_f32_16x16x32_bf16 v[128:131], %[ones], v[196:199], v[128:131]\n\t" \
  "v_cvt_pk_bf16_f32 v200, v40, v41\n\t" \
  "v_cvt_pk_bf16_f32 v201, v42, v43\n\t" \
  "v_cvt_pk_bf16_f32 v202, v44, v45\n\t" \
  "s_mov_b32 m0, %[wm]\n\t" \
  "s_add_u32 %[wm], %[wm], 0x400\n\t" \
  "global_load_lds_dwordx4 %[wv], %[wb1]\n\t" \
  "v_add_u32 %[wv], 0x4000, %[wv]\n\t" \
  "v_cvt_pk_bf16_f32 v203, v46, v47\n\t" \
  "v_exp_f32 v48, v48\n\t" \
  "v_exp_f32 v49, v49\n\t" \
  "s_waitcnt lgkmcnt(4)\n\t" \
  "v_mfma_f32_32x32x16_bf16 v[112:127], v[176:179], v[200:203], v[112:127]\n\t" \
  "v_exp_f32 v50, v50\n\t" \
  "v_exp_f32 v51, v51\n\t" \
  "v_mfma_f32_16x16x32_bf16 v[128:131], %[ones], v[200:203], v[128:131]\n\t" \
  "v_exp_f32 v52, v52\n\t" \
  "v_exp_f32 v53, v53\n\t" \
  "s_mov_b32 m0, %[wm]\n\t" \
  "s_add_u32 %[wm], %[wm], 0x400\n\t" \
  "global_load_lds_dwordx4 %[wv], %[wb1]\n\t" \
  "v_add_u32 %[wv], 0x4000, %[wv]\n\t" \
  "v_exp_f32 v54, v54\n\t" \
  "s_waitcnt lgkmcnt(3)\n\t" \
  "s_cmp_eq_i32 %[i0], 6\n\t" \
  "s_cbranch_scc1 48f\n\t" \
  "49:\n\t" \
  "v_mfma_f32_32x32x16_bf16 v[32:47], v[148:151], %[q0], v[96:111]\n\t" \
  "v_exp_f32 v55, v55\n\t" \
  "v_cvt_pk_bf16_f32 v196, v48, v49\n\t" \
  "v_cvt_pk_bf16_f32 v197, v50, v51\n\t" \
  "v_cvt_pk_bf16_f32 v198, v52, v53\n\t" \
  "v_cvt_pk_bf16_f32 v199, v54, v55\n\t" \
  "s_waitcnt lgkmcnt(2)\n\t" \
  "v_mfma_f32_32x32x16_bf16 v[32:47], v[152:155], %[q1], v[32:47]\n\t" \
  "v_exp_f32 v56, v56\n\t" \
  "ds_read_b128 v[156:159], %[ka] offset:28672\n\t" \
  "v_exp_f32 v57, v57\n\t" \
  "ds_read_b128 v[160:163], %[ka] offset:29184\n\t" \
  "v_exp_f32 v58, v58\n\t" \
  "v_exp_f32 v59, v59\n\t" \
  "s_waitcnt lgkmcnt(3)\n\t" \
  "v_mfma_f32_32x32x16_bf16 v[112:127], v[180:183], v[196:199], v[112:127]\n\t" \
  "v_exp_f32 v60, v60\n\t" \
  "ds_read_b128 v[188:191], %[va] offset:24576\n\t" \
  "v_exp_f32 v61, v61\n\t" \
  "ds_read_b128 v[192:195], %[va] offset:25088\n\t" \
  "v_exp_f32 v62, v62\n\t" \
  "v_exp_f32 v63, v63\n\t" \
  "v_mfma_f32_16x16x32_bf16 v[128:131], %[ones], v[196:199], v[128:131]\n\t" \
  "v_cvt_pk_bf16_f32 v200, v56, v57\n\t" \
  "v_cvt_pk_bf16_f32 v201, v58, v59\n\t" \
  "v_cvt_pk_bf16_f32 v202, v60, v61\n\t" \
  "s_mov_b32 m0, %[wm]\n\t" \
  "s_add_u32 %[wm], %[wm], 0x400\n\t" \
  "global_load_lds_dwordx4 %[wv], %[wb1]\n\t" \
  "v_add_u32 %[wv], 0x4000, %[wv]\n\t" \
  "v_cvt_pk_bf16_f32 v203, v62, v63\n\t" \
  "v_exp_f32 v32, v32\n\t" \
  "v_exp_f32 v33, v33\n\t" \
  "s_waitcnt lgkmcnt(4)\n\t" \
  "v_mfma_f32_32x32x16_bf16 v[112:127], v[184:187], v[200:203], v[112:127]\n\t" \
  "v_exp_f32 v34, v34\n\t" \
  "v_exp_f32 v35, v35\n\t" \
  "v_mfma_f32_16x16x32_bf16 v[128:131], %[ones], v[200:203], v[128:131]\n\t" \
  "v_exp_f32 v36, v36\n\t" \
  "v_exp_f32 v37, v37\n\t" \
  "s_cmp_lg_u32 %[par], 0\n\t" \
  "s_cbranch_scc1 96f\n\t" \
  "global_load_dwordx2 %[pk], %[pko], %[wsb] sc1\n\t" \
  "96:\n\t" \
  "v_exp_f32 v38, v38\n\t" \
  "s_waitcnt lgkmcnt(3)\n\t" \
  "s_cmp_eq_i32 %[i0], 7\n\t" \
  "s_cbranch_scc1 50f\n\t" \
  "51:\n\t" \
  "v_mfma_f32_32x32x16_bf16 v[48:63], v[156:159], %[q0], v[96:111]\n\t" \
  "v_exp_f32 v39, v39\n\t" \
  "v_cvt_pk_bf16_f32 v196, v32, v33\n\t" \
  "v_cvt_pk_bf16_f32 v197, v34, v35\n\t" \
  "v_cvt_pk_bf16_f32 v198, v36, v37\n\t" \
  "v_cvt_pk_bf16_f32 v199, v38, v39\n\t" \
  "s_waitcnt lgkmcnt(2)\n\t" \
  "v_mfma_f32_32x32x16_bf16 v[48:63], v[160:163], %[q1], v[48:63]\n\t" \
  "v_exp_f32 v40, v40\n\t" \
  "ds_read_b128 v[164:167], %[va] offset:28672\n\t" \
  "v_exp_f32 v41, v41\n\t" \
  "ds_read_b128 v[168:171], %[va] offset:29184\n\t" \
  "v_exp_f32 v42, v42\n\t" \
  "v_exp_f32 v43, v43\n\t" \
  "s_waitcnt lgkmcnt(3)\n\t" \
  "v_mfma_f32_32x32x16_bf16 v[112:127], v[188:191], v[196:199], v[112:127]\n\t" \
  "v_exp_f32 v44, v44\n\t" \
  "v_exp_f32 v45, v45\n\t" \
  "v_exp_f32 v46, v46\n\t" \
  "v_exp_f32 v47, v47\n\t" \
  "v_mfma_f32_16x16x32_bf16 v[128:131], %[ones], v[196:199], v[128:131]\n\t" \
  "v_cvt_pk_bf16_f32 v200, v40, v41\n\t" \
  "v_cvt_pk_bf16_f32 v201, v42, v43\n\t" \
  "v_cvt_pk_bf16_f32 v202, v44, v45\n\t" \
  "v_cvt_pk_bf16_f32 v203, v46, v47\n\t" \
  "v_exp_f32 v48, v48\n\t" \
  "v_exp_f32 v49, v49\n\t" \
  "s_waitcnt lgkmcnt(2)\n\t" \
  "v_mfma_f32_32x32x16_bf16 v[112:127], v[192:195], v[200:203], v[112:127]\n\t" \
  "v_exp_f32 v50, v50\n\t" \
  "v_exp_f32 v51, v51\n\t" \
  "v_mfma_f32_16x16x32_bf16 v[128:131], %[ones], v[200:203], v[128:131]\n\t" \
  "v_exp_f32 v52, v52\n\t" \
  "v_exp_f32 v53, v53\n\t" \
  "v_exp_f32 v54, v54\n\t" \
  "v_exp_f32 v55, v55\n\t" \
  "v_cvt_pk_bf16_f32 v196, v48, v49\n\t" \
  "v_cvt_pk_bf16_f32 v197, v50, v51\n\t" \
  "v_cvt_pk_bf16_f32 v198, v52, v53\n\t" \
  "v_cvt_pk_bf16_f32 v199, v54, v55\n\t" \
  "v_exp_f32 v56, v56\n\t" \
  "v_exp_f32 v57, v57\n\t" \
  "v_exp_f32 v58, v58\n\t" \
  "v_exp_f32 v59, v59\n\t" \
  "s_waitcnt lgkmcnt(1)\n\t" \
  "v_mfma_f32_32x32x16_bf16 v[112:127], v[164:167], v[196:199], v[112:127]\n\t" \
  "v_exp_f32 v60, v60\n\t" \
  "v_exp_f32 v61, v61\n\t" \
  "v_exp_f32 v62, v62\n\t" \
  "v_exp_f32 v63, v63\n\t" \
  "v_mfma_f32_16x16x32_bf16 v[128:131], %[ones], v[196:199], v[128:131]\n\t" \
  "v_cvt_pk_bf16_f32 v200, v56, v57\n\t" \
  "v_cvt_pk_bf16_f32 v201, v58, v59\n\t" \
  "v_cvt_pk_bf16_f32 v202, v60, v61\n\t" \
  "v_cvt_pk_bf16_f32 v203, v62, v63\n\t" \
  "s_waitcnt lgkmcnt(0)\n\t" \
  "s_nop 0\n\t" \
  "v_mfma_f32_32x32x16_bf16 v[112:127], v[168:171], v[200:203], v[112:127]\n\t" \
  "v_mfma_f32_16x16x32_bf16 v[128:131], %[ones], v[200:203], v[128:131]\n\t" \
  "s_nop 7\n\t" \
  "s_nop 3\n\t" \
  "s_branch 99f\n\t" \
  "40:\n\t" \
  "v_mov_b32 v96, v212\n\t" \
  "v_mov_b32 v97, v212\n\t" \
  "v_mov_b32 v98, v212\n\t" \
  "v_mov_b32 v99, v212\n\t" \
  "v_mov_b32 v100, v212\n\t" \
  "v_mov_b32 v101, v212\n\t" \
  "v_mov_b32 v102, v212\n\t" \
  "v_mov_b32 v103, v212\n\t" \
  "v_mov_b32 v104, v212\n\t" \
  "v_mov_b32 v105, v212\n\t" \
  "v_mov_b32 v106, v212\n\t" \
  "v_mov_b32 v107, v212\n\t" \
  "v_mov_b32 v108, v212\n\t" \
  "v_mov_b32 v109, v212\n\t" \
  "v_mov_b32 v110, v212\n\t" \
  "v_mov_b32 v111, v212\n\t" \
  "s_nop 0\n\t" \
  "s_branch 41b\n\t" \
  "42:\n\t" \
  "v_mov_b32 v96, v212\n\t" \
  "v_mov_b32 v97, v212\n\t" \
  "v_mov_b32 v98, v212\n\t" \
  "v_mov_b32 v99, v212\n\t" \
  "v_mov_b32 v100, v212\n\t" \
  "v_mov_b32 v101, v212\n\t" \
  "v_mov_b32 v102, v212\n\t" \
  "v_mov_b32 v103, v212\n\t" \
  "v_mov_b32 v104, v212\n\t" \
  "v_mov_b32 v105, v212\n\t" \
  "v_mov_b32 v106, v212\n\t" \
  "v_mov_b32 v107, v212\n\t" \
  "v_mov_b32 v108, v212\n\t" \
  "v_mov_b32 v109, v212\n\t" \
  "v_mov_b32 v110, v212\n\t" \
  "v_mov_b32 v111, v212\n\t" \
  "s_nop 0\n\t" \
  "s_branch 43b\n\t" \
  "44:\n\t" \
  "v_mov_b32 v96, v212\n\t" \
  "v_mov_b32 v97, v212\n\t" \
  "v_mov_b32 v98, v212\n\t" \
  "v_mov_b32 v99, v212\n\t" \
  "v_mov_b32 v100, v212\n\t" \
  "v_mov_b32 v101, v212\n\t" \
  "v_mov_b32 v102, v212\n\t" \
  "v_mov_b32 v103, v212\n\t" \
  "v_mov_b32 v104, v212\n\t" \
  "v_mov_b32 v105, v212\n\t" \
  "v_mov_b32 v106, v212\n\t" \
  "v_mov_b32 v107, v212\n\t" \
  "v_mov_b32 v108, v212\n\t" \
  "v_mov_b32 v109, v212\n\t" \
  "v_mov_b32 v110, v212\n\t" \
  "v_mov_b32 v111, v212\n\t" \
  "s_nop 0\n\t" \
  "s_branch 45b\n\t" \
  "46:\n\t" \
  "v_mov_b32 v96, v212\n\t" \
  "v_mov_b32 v97, v212\n\t" \
  "v_mov_b32 v98, v212\n\t" \
  "v_mov_b32 v99, v212\n\t" \
  "v_mov_b32 v100, v212\n\t" \
  "v_mov_b32 v101, v212\n\t" \
  "v_mov_b32 v102, v212\n\t" \
  "v_mov_b32 v103, v212\n\t" \
  "v_mov_b32 v104, v212\n\t" \
  "v_mov_b32 v105, v212\n\t" \
  "v_mov_b32 v106, v212\n\t" \
  "v_mov_b32 v107, v212\n\t" \
  "v_mov_b32 v108, v212\n\t" \
  "v_mov_b32 v109, v212\n\t" \
  "v_mov_b32 v110, v212\n\t" \
  "v_mov_b32 v111, v212\n\t" \
  "s_nop 0\n\t" \
  "s_branch 47b\n\t" \
  "48:\n\t" \
  "v_mov_b32 v96, v212\n\t" \
  "v_mov_b32 v97, v212\n\t" \
  "v_mov_b32 v98, v212\n\t" \
  "v_mov_b32 v99, v212\n\t" \
  "v_mov_b32 v100, v212\n\t" \
  "v_mov_b32 v101, v212\n\t" \
  "v_mov_b32 v102, v212\n\t" \
  "v_mov_b32 v103, v212\n\t" \
  "v_mov_b32 v104, v212\n\t" \
  "v_mov_b32 v105, v212\n\t" \
  "v_mov_b32 v106, v212\n\t" \
  "v_mov_b32 v107, v212\n\t" \
  "v_mov_b32 v108, v212\n\t" \
  "v_mov_b32 v109, v212\n\t" \
  "v_mov_b32 v110, v212\n\t" \
  "v_mov_b32 v111, v212\n\t" \
  "s_nop 0\n\t" \
  "s_branch 49b\n\t" \
  "50:\n\t" \
  "v_mov_b32 v96, v212\n\t" \
  "v_mov_b32 v97, v212\n\t" \
  "v_mov_b32 v98, v212\n\t" \
  "v_mov_b32 v99, v212\n\t" \
  "v_mov_b32 v100, v212\n\t" \
  "v_mov_b32 v101, v212\n\t" \
  "v_mov_b32 v102, v212\n\t" \
  "v_mov_b32 v103, v212\n\t" \
  "v_mov_b32 v104, v212\n\t" \
  "v_mov_b32 v105, v212\n\t" \
  "v_mov_b32 v106, v212\n\t" \
  "v_mov_b32 v107, v212\n\t" \
  "v_mov_b32 v108, v212\n\t" \
  "v_mov_b32 v109, v212\n\t" \
  "v_mov_b32 v110, v212\n\t" \
  "v_mov_b32 v111, v212\n\t" \
  "s_nop 0\n\t" \
  "s_branch 51b\n\t" \
  "99:\n\t"
#define ATT2_CLOB "v32","v33","v34","v35","v36","v37","v38","v39","v40","v41","v42","v43","v44","v45","v46","v47","v48","v49","v50","v51","v52","v53","v54","v55","v56","v57","v58","v59","v60","v61","v62","v63","v64","v65","v66","v67","v68","v69","v70","v71","v96","v97","v98","v99","v100","v101","v102","v103","v104","v105","v106","v107","v108","v109","v110","v111","v132","v133","v134","v135","v136","v137","v138","v139","v140","v141","v142","v143","v144","v145","v146","v147","v148","v149","v150","v151","v152","v153","v154","v155","v156","v157","v158","v159","v160","v161","v162","v163","v164","v165","v166","v167","v168","v169","v170","v171","v172","v173","v174","v175","v176","v177","v178","v179","v180","v181","v182","v183","v184","v185","v186","v187","v188","v189","v190","v191","v192","v193","v194","v195","v196","v197","v198","v199","v200","v201","v202","v203","v204","v205","v206","v207","v208","v209","v210","v211","v212","scc"
#define CONV_ASM \
  "ds_read_b64_tr_b16 v[24:25], %[ha] offset:0\n\t" \
  "ds_read_b64_tr_b16 v[26:27], %[ha] offset:32\n\t" \
  "ds_read_b64 v[28:29], %[xa] offset:0\n\t" \
  "ds_read_b64 v[30:31], %[xa] offset:8\n\t" \
  "ds_read_b64 v[32:33], %[xa] offset:4672\n\t" \
  "ds_read_b64 v[34:35], %[xa] offset:4680\n\t" \
  "ds_read_b64_tr_b16 v[36:37], %[ha] offset:256\n\t" \
  "ds_read_b64_tr_b16 v[38:39], %[ha] offset:288\n\t" \
  "ds_read_b64 v[40:41], %[xa] offset:64\n\t" \
  "ds_read_b64 v[42:43], %[xa] offset:72\n\t" \
  "ds_read_b64 v[44:45], %[xa] offset:4736\n\t" \
  "ds_read_b64 v[46:47], %[xa] offset:4744\n\t" \
  "ds_read_b64 v[48:49], %[xa] offset:128\n\t" \
  "ds_read_b64 v[50:51], %[xa] offset:136\n\t" \
  "s_waitcnt lgkmcnt(10)\n\t" \
  "v_mfma_f32_16x16x32_bf16 v[156:159], v[24:27], v[28:31], 0\n\t" \
  "ds_read_b64_tr_b16 v[52:53], %[ha] offset:512\n\t" \
  "ds_read_b64_tr_b16 v[54:55], %[ha] offset:544\n\t" \
  "s_waitcnt lgkmcnt(10)\n\t" \
  "v_mfma_f32_16x16x32_bf16 v[188:191], v[24:27], v[32:35], 0\n\t" \
  "ds_read_b64 v[56:57], %[xa] offset:4800\n\t" \
  "ds_read_b64 v[58:59], %[xa] offset:4808\n\t" \
  "s_waitcnt lgkmcnt(8)\n\t" \
  "v_mfma_f32_16x16x32_bf16 v[156:159], v[36:39], v[40:43], v[156:159]\n\t" \
  "ds_read_b64 v[60:61], %[xa] offset:192\n\t" \
  "ds_read_b64 v[62:63], %[xa] offset:200\n\t" \
  "s_waitcnt lgkmcnt(8)\n\t" \
  "v_mfma_f32_16x16x32_bf16 v[188:191], v[36:39], v[44:47], v[188:191]\n\t" \
  "ds_read_b64_tr_b16 v[64:65], %[ha] offset:768\n\t" \
  "ds_read_b64_tr_b16 v[66:67], %[ha] offset:800\n\t" \
  "s_waitcnt lgkmcnt(8)\n\t" \
  "v_mfma_f32_16x16x32_bf16 v[152:155], v[24:27], v[48:51], 0\n\t" \
  "ds_read_b64 v[68:69], %[xa] offset:4864\n\t" \
  "ds_read_b64 v[70:71], %[xa] offset:4872\n\t" \
  "s_waitcnt lgkmcnt(8)\n\t" \
  "v_mfma_f32_16x16x32_bf16 v[156:159], v[52:55], v[48:51], v[156:159]\n\t" \
  "ds_read_b64 v[72:73], %[xa] offset:256\n\t" \
  "ds_read_b64 v[74:75], %[xa] offset:264\n\t" \
  "s_waitcnt lgkmcnt(8)\n\t" \
  "v_mfma_f32_16x16x32_bf16 v[184:187], v[24:27], v[56:59], 0\n\t" \
  "ds_read_b64_tr_b16 v[76:77], %[ha] offset:1024\n\t" \
  "s_barrier\n\t" \
  "v_mfma_f32_16x16x32_bf16 v[188:191], v[52:55], v[56:59], v[188:191]\n\t" \
  "ds_read_b64_tr_b16 v[78:79], %[ha] offset:1056\n\t" \
  "ds_read_b64 v[80:81], %[xa] offset:4928\n\t" \
  "s_waitcnt lgkmcnt(9)\n\t" \
  "v_mfma_f32_16x16x32_bf16 v[152:155], v[36:39], v[60:63], v[152:155]\n\t" \
  "ds_read_b64 v[82:83], %[xa] offset:4936\n\t" \
  "ds_read_b64 v[84:85], %[xa] offset:320\n\t" \
  "s_waitcnt lgkmcnt(9)\n\t" \
  "v_mfma_f32_16x16x32_bf16 v[156:159], v[64:67], v[60:63], v[156:159]\n\t" \
  "ds_read_b64 v[86:87], %[xa] offset:328\n\t" \
  "ds_read_b64_tr_b16 v[88:89], %[ha] offset:1280\n\t" \
  "s_waitcnt lgkmcnt(9)\n\t" \
  "v_mfma_f32_16x16x32_bf16 v[184:187], v[36:39], v[68:71], v[184:187]\n\t" \
  "ds_read_b64_tr_b16 v[90:91], %[ha] offset:1312\n\t" \
  "ds_read_b64 v[92:93], %[xa] offset:4992\n\t" \
  "v_mfma_f32_16x16x32_bf16 v[188:191], v[64:67], v[68:71], v[188:191]\n\t" \
  "ds_read_b64 v[94:95], %[xa] offset:5000\n\t" \
  "ds_read_b64 v[96:97], %[xa] offset:384\n\t" \
  "s_waitcnt lgkmcnt(11)\n\t" \
  "v_mfma_f32_16x16x32_bf16 v[148:151], v[24:27], v[72:75], 0\n\t" \
  "ds_read_b64 v[98:99], %[xa] offset:392\n\t" \
  "ds_write_b128 %[lx], v[160:163] offset:2048\n\t" \
  "v_mfma_f32_16x16x32_bf16 v[152:155], v[52:55], v[72:75], v[152:155]\n\t" \
  "ds_read_b64_tr_b16 v[100:101], %[ha] offset:1536\n\t" \
  "ds_read_b64_tr_b16 v[102:103], %[ha] offset:1568\n\t" \
  "s_waitcnt lgkmcnt(13)\n\t" \
  "v_mfma_f32_16x16x32_bf16 v[156:159], v[76:79], v[72:75], v[156:159]\n\t" \
  "ds_read_b64 v[104:105], %[xa] offset:5056\n\t" \
  "ds_read_b64 v[106:107], %[xa] offset:5064\n\t" \
  "s_waitcnt lgkmcnt(13)\n\t" \
  "v_mfma_f32_16x16x32_bf16 v[180:183], v[24:27], v[80:83], 0\n\t" \
  "ds_read_b64 v[108:109], %[xa] offset:448\n\t" \
  "ds_write_b128 %[lx], v[164:167] offset:3072\n\t" \
  "v_mfma_f32_16x16x32_bf16 v[184:187], v[52:55], v[80:83], v[184:187]\n\t" \
  "v_mfma_f32_16x16x32_bf16 v[188:191], v[76:79], v[80:83], v[188:191]\n\t" \
  "s_waitcnt lgkmcnt(13)\n\t" \
  "v_mfma_f32_16x16x32_bf16 v[148:151], v[36:39], v[84:87], v[148:151]\n\t" \
  "ds_read_b64 v[110:111], %[xa] offset:456\n\t" \
  "ds_write_b128 %[lx], v[168:171] offset:6144\n\t" \
  "v_mfma_f32_16x16x32_bf16 v[152:155], v[64:67], v[84:87], v[152:155]\n\t" \
  "ds_read_b64_tr_b16 v[112:113], %[ha] offset:1792\n\t" \
  "s_waitcnt lgkmcnt(14)\n\t" \
  "v_mfma_f32_16x16x32_bf16 v[156:159], v[88:91], v[84:87], v[156:159]\n\t" \
  "ds_read_b64_tr_b16 v[114:115], %[ha] offset:1824\n\t" \
  "ds_read_b64 v[116:117], %[xa] offset:5120\n\t" \
  "s_waitcnt lgkmcnt(14)\n\t" \
  "v_mfma_f32_16x16x32_bf16 v[180:183], v[36:39], v[92:95], v[180:183]\n\t" \
  "ds_read_b64 v[118:119], %[xa] offset:5128\n\t" \
  "ds_write_b128 %[lx], v[172:175] offset:7168\n\t" \
  "v_mfma_f32_16x16x32_bf16 v[184:187], v[64:67], v[92:95], v[184:187]\n\t" \
  "ds_read_b64 v[120:121], %[xa] offset:512\n\t" \
  "ds_read_b64 v[122:123], %[xa] offset:520\n\t" \
  "v_mfma_f32_16x16x32_bf16 v[188:191], v[88:91], v[92:95], v[188:191]\n\t" \
  "ds_read_b64_tr_b16 v[124:125], %[ha] offset:2048\n\t" \
  "ds_read_b64_tr_b16 v[126:127], %[ha] offset:2080\n\t" \
  "s_waitcnt lgkmcnt(15)\n\t" \
  "v_mfma_f32_16x16x32_bf16 v[144:147], v[24:27], v[96:99], 0\n\t" \
  "ds_read_b64 v[28:29], %[xa] offset:5184\n\t" \
  "ds_read_b64 v[30:31], %[xa] offset:5192\n\t" \
  "v_mfma_f32_16x16x32_bf16 v[148:151], v[52:55], v[96:99], v[148:151]\n\t" \
  "ds_read_b64 v[32:33], %[xa] offset:576\n\t" \
  "ds_read_b64 v[34:35], %[xa] offset:584\n\t" \
  "v_mfma_f32_16x16x32_bf16 v[152:155], v[76:79], v[96:99], v[152:155]\n\t" \
  "v_mfma_f32_16x16x32_bf16 v[156:159], v[100:103], v[96:99], v[156:159]\n\t" \
  "s_waitcnt lgkmcnt(15)\n\t" \
  "v_mfma_f32_16x16x32_bf16 v[176:179], v[24:27], v[104:107], 0\n\t" \
  "ds_read_b64_tr_b16 v[40:41], %[ha] offset:2304\n\t" \
  "v_mfma_f32_16x16x32_bf16 v[180:183], v[52:55], v[104:107], v[180:183]\n\t" \
  "ds_read_b64_tr_b16 v[42:43], %[ha] offset:2336\n\t" \
  "ds_read_b64 v[44:45], %[xa] offset:5248\n\t" \
  "v_mfma_f32_16x16x32_bf16 v[184:187], v[76:79], v[104:107], v[184:187]\n\t" \
  "ds_read_b64 v[46:47], %[xa] offset:5256\n\t" \
  "ds_read_b64 v[48:49], %[xa] offset:640\n\t" \
  "v_mfma_f32_16x16x32_bf16 v[188:191], v[100:103], v[104:107], v[188:191]\n\t" \
  "ds_read_b64 v[50:51], %[xa] offset:648\n\t" \
  "ds_read_b64_tr_b16 v[56:57], %[ha] offset:2560\n\t" \
  "s_waitcnt lgkmcnt(15)\n\t" \
  "v_mfma_f32_16x16x32_bf16 v[144:147], v[36:39], v[108:111], v[144:147]\n\t" \
  "ds_read_b64_tr_b16 v[58:59], %[ha] offset:2592\n\t" \
  "v_mfma_f32_16x16x32_bf16 v[148:151], v[64:67], v[108:111], v[148:151]\n\t" \
  "ds_read_b64 v[60:61], %[xa] offset:5312\n\t" \
  "ds_read_b64 v[62:63], %[xa] offset:5320\n\t" \
  "v_mfma_f32_16x16x32_bf16 v[152:155], v[88:91], v[108:111], v[152:155]\n\t" \
  "ds_read_b64 v[68:69], %[xa] offset:704\n\t" \
  "ds_read_b64 v[70:71], %[xa] offset:712\n\t" \
  "v_mfma_f32_16x16x32_bf16 v[156:159], v[112:115], v[108:111], v[156:159]\n\t" \
  "ds_read_b64_tr_b16 v[72:73], %[ha] offset:2816\n\t" \
  "ds_read_b64_tr_b16 v[74:75], %[ha] offset:2848\n\t" \
  "v_mfma_f32_16x16x32_bf16 v[176:179], v[36:39], v[116:119], v[176:179]\n\t" \
  "ds_read_b64 v[80:81], %[xa] offset:5376\n\t" \
  "v_mfma_f32_16x16x32_bf16 v[180:183], v[64:67], v[116:119], v[180:183]\n\t" \
  "v_mfma_f32_16x16x32_bf16 v[184:187], v[88:91], v[116:119], v[184:187]\n\t" \
  "ds_read_b64 v[82:83], %[xa] offset:5384\n\t" \
  "v_mfma_f32_16x16x32_bf16 v[188:191], v[112:115], v[116:119], v[188:191]\n\t" \
  "ds_read_b64 v[84:85], %[xa] offset:768\n\t" \
  "ds_read_b64 v[86:87], %[xa] offset:776\n\t" \
  "s_waitcnt lgkmcnt(15)\n\t" \
  "v_mfma_f32_16x16x32_bf16 v[140:143], v[24:27], v[120:123], 0\n\t" \
  "v_mfma_f32_16x16x32_bf16 v[144:147], v[52:55], v[120:123], v[144:147]\n\t" \
  "v_mfma_f32_16x16x32_bf16 v[148:151], v[76:79], v[120:123], v[148:151]\n\t" \
  "ds_read_b64_tr_b16 v[92:93], %[ha] offset:3072\n\t" \
  "ds_read_b64_tr_b16 v[94:95], %[ha] offset:3104\n\t" \
  "v_mfma_f32_16x16x32_bf16 v[152:155], v[100:103], v[120:123], v[152:155]\n\t" \
  "ds_read_b64 v[96:97], %[xa] offset:5440\n\t" \
  "ds_read_b64 v[98:99], %[xa] offset:5448\n\t" \
  "v_mfma_f32_16x16x32_bf16 v[156:159], v[124:127], v[120:123], v[156:159]\n\t" \
  "v_mfma_f32_16x16x32_bf16 v[172:175], v[24:27], v[28:31], 0\n\t" \
  "v_mfma_f32_16x16x32_bf16 v[176:179], v[52:55], v[28:31], v[176:179]\n\t" \
  "v_mfma_f32_16x16x32_bf16 v[180:183], v[76:79], v[28:31], v[180:183]\n\t" \
  "v_mfma_f32_16x16x32_bf16 v[184:187], v[100:103], v[28:31], v[184:187]\n\t" \
  "v_mfma_f32_16x16x32_bf16 v[188:191], v[124:127], v[28:31], v[188:191]\n\t" \
  "v_mfma_f32_16x16x32_bf16 v[140:143], v[36:39], v[32:35], v[140:143]\n\t" \
  "ds_read_b64 v[104:105], %[xa] offset:832\n\t" \
  "ds_read_b64 v[106:107], %[xa] offset:840\n\t" \
  "v_mfma_f32_16x16x32_bf16 v[144:147], v[64:67], v[32:35], v[144:147]\n\t" \
  "v_mfma_f32_16x16x32_bf16 v[148:151], v[88:91], v[32:35], v[148:151]\n\t" \
  "v_mfma_f32_16x16x32_bf16 v[152:155], v[112:115], v[32:35], v[152:155]\n\t" \
  "v_mfma_f32_16x16x32_bf16 v[156:159], v[40:43], v[32:35], v[156:159]\n\t" \
  "s_waitcnt lgkmcnt(15)\n\t" \
  "v_mfma_f32_16x16x32_bf16 v[172:175], v[36:39], v[44:47], v[172:175]\n\t" \
  "v_mfma_f32_16x16x32_bf16 v[176:179], v[64:67], v[44:47], v[176:179]\n\t" \
  "ds_read_b64_tr_b16 v[108:109], %[ha] offset:3328\n\t" \
  "ds_read_b64_tr_b16 v[110:111], %[ha] offset:3360\n\t" \
  "v_mfma_f32_16x16x32_bf16 v[180:183], v[88:91], v[44:47], v[180:183]\n\t" \
  "ds_read_b64 v[116:117], %[xa] offset:5504\n\t" \
  "ds_read_b64 v[118:119], %[xa] offset:5512\n\t" \
  "v_mfma_f32_16x16x32_bf16 v[184:187], v[112:115], v[44:47], v[184:187]\n\t" \
  "v_mfma_f32_16x16x32_bf16 v[188:191], v[40:43], v[44:47], v[188:191]\n\t" \
  "v_mfma_f32_16x16x32_bf16 v[136:139], v[24:27], v[48:51], 0\n\t" \
  "v_mfma_f32_16x16x32_bf16 v[140:143], v[52:55], v[48:51], v[140:143]\n\t" \
  "v_mfma_f32_16x16x32_bf16 v[144:147], v[76:79], v[48:51], v[144:147]\n\t" \
  "v_mfma_f32_16x16x32_bf16 v[148:151], v[100:103], v[48:51], v[148:151]\n\t" \
  "v_mfma_f32_16x16x32_bf16 v[152:155], v[124:127], v[48:51], v[152:155]\n\t" \
  "ds_read_b64 v[120:121], %[xa] offset:896\n\t" \
  "ds_read_b64 v[122:123], %[xa] offset:904\n\t" \
  "v_mfma_f32_16x16x32_bf16 v[156:159], v[56:59], v[48:51], v[156:159]\n\t" \
  "s_waitcnt lgkmcnt(15)\n\t" \
  "v_mfma_f32_16x16x32_bf16 v[168:171], v[24:27], v[60:63], 0\n\t" \
  "v_mfma_f32_16x16x32_bf16 v[172:175], v[52:55], v[60:63], v[172:175]\n\t" \
  "v_mfma_f32_16x16x32_bf16 v[176:179], v[76:79], v[60:63], v[176:179]\n\t" \
  "v_mfma_f32_16x16x32_bf16 v[180:183], v[100:103], v[60:63], v[180:183]\n\t" \
  "v_mfma_f32_16x16x32_bf16 v[184:187], v[124:127], v[60:63], v[184:187]\n\t" \
  "v_mfma_f32_16x16x32_bf16 v[188:191], v[56:59], v[60:63], v[188:191]\n\t" \
  "ds_read_b64_tr_b16 v[28:29], %[ha] offset:3584\n\t" \
  "ds_read_b64_tr_b16 v[30:31], %[ha] offset:3616\n\t" \
  "v_mfma_f32_16x16x32_bf16 v[136:139], v[36:39], v[68:71], v[136:139]\n\t" \
  "ds_read_b64 v[32:33], %[xa] offset:5568\n\t" \
  "ds_read_b64 v[34:35], %[xa] offset:5576\n\t" \
  "v_mfma_f32_16x16x32_bf16 v[140:143], v[64:67], v[68:71], v[140:143]\n\t" \
  "v_mfma_f32_16x16x32_bf16 v[144:147], v[88:91], v[68:71], v[144:147]\n\t" \
  "v_mfma_f32_16x16x32_bf16 v[148:151], v[112:115], v[68:71], v[148:151]\n\t" \
  "v_mfma_f32_16x16x32_bf16 v[152:155], v[40:43], v[68:71], v[152:155]\n\t" \
  "v_mfma_f32_16x16x32_bf16 v[156:159], v[72:75], v[68:71], v[156:159]\n\t" \
  "s_waitcnt lgkmcnt(15)\n\t" \
  "v_mfma_f32_16x16x32_bf16 v[168:171], v[36:39], v[80:83], v[168:171]\n\t" \
  "v_mfma_f32_16x16x32_bf16 v[172:175], v[64:67], v[80:83], v[172:175]\n\t" \
  "v_mfma_f32_16x16x32_bf16 v[176:179], v[88:91], v[80:83], v[176:179]\n\t" \
  "ds_read_b64 v[44:45], %[xa] offset:960\n\t" \
  "ds_read_b64 v[46:47], %[xa] offset:968\n\t" \
  "v_mfma_f32_16x16x32_bf16 v[180:183], v[112:115], v[80:83], v[180:183]\n\t" \
  "v_mfma_f32_16x16x32_bf16 v[184:187], v[40:43], v[80:83], v[184:187]\n\t" \
  "v_mfma_f32_16x16x32_bf16 v[188:191], v[72:75], v[80:83], v[188:191]\n\t" \
  "v_mfma_f32_16x16x32_bf16 v[132:135], v[24:27], v[84:87], 0\n\t" \
  "v_mfma_f32_16x16x32_bf16 v[136:139], v[52:55], v[84:87], v[136:139]\n\t" \
  "v_mfma_f32_16x16x32_bf16 v[140:143], v[76:79], v[84:87], v[140:143]\n\t" \
  "v_mfma_f32_16x16x32_bf16 v[144:147], v[100:103], v[84:87], v[144:147]\n\t" \
  "ds_read_b64_tr_b16 v[48:49], %[ha] offset:3840\n\t" \
  "ds_read_b64_tr_b16 v[50:51], %[ha] offset:3872\n\t" \
  "v_mfma_f32_16x16x32_bf16 v[148:151], v[124:127], v[84:87], v[148:151]\n\t" \
  "ds_read_b64 v[60:61], %[xa] offset:5632\n\t" \
  "ds_read_b64 v[62:63], %[xa] offset:5640\n\t" \
  "v_mfma_f32_16x16x32_bf16 v[152:155], v[56:59], v[84:87], v[152:155]\n\t" \
  "s_waitcnt lgkmcnt(15)\n\t" \
  "v_mfma_f32_16x16x32_bf16 v[156:159], v[92:95], v[84:87], v[156:159]\n\t" \
  "v_mfma_f32_16x16x32_bf16 v[164:167], v[24:27], v[96:99], 0\n\t" \
  "v_mfma_f32_16x16x32_bf16 v[168:171], v[52:55], v[96:99], v[168:171]\n\t" \
  "v_mfma_f32_16x16x32_bf16 v[172:175], v[76:79], v[96:99], v[172:175]\n\t" \
  "v_mfma_f32_16x16x32_bf16 v[176:179], v[100:103], v[96:99], v[176:179]\n\t" \
  "v_mfma_f32_16x16x32_bf16 v[180:183], v[124:127], v[96:99], v[180:183]\n\t" \
  "v_mfma_f32_16x16x32_bf16 v[184:187], v[56:59], v[96:99], v[184:187]\n\t" \
  "ds_read_b64_tr_b16 v[68:69], %[ha] offset:7968\n\t" \
  "ds_read_b64_tr_b16 v[70:71], %[ha] offset:8000\n\t" \
  "v_mfma_f32_16x16x32_bf16 v[188:191], v[92:95], v[96:99], v[188:191]\n\t" \
  "ds_read_b64 v[80:81], %[xa] offset:10304\n\t" \
  "ds_read_b64 v[82:83], %[xa] offset:10312\n\t" \
  "v_mfma_f32_16x16x32_bf16 v[132:135], v[36:39], v[104:107], v[132:135]\n\t" \
  "ds_read_b64_tr_b16 v[84:85], %[ha] offset:7456\n\t" \
  "ds_read_b64_tr_b16 v[86:87], %[ha] offset:7488\n\t" \
  "v_mfma_f32_16x16x32_bf16 v[136:139], v[64:67], v[104:107], v[136:139]\n\t" \
  "ds_read_b64_tr_b16 v[96:97], %[ha] offset:6944\n\t" \
  "ds_read_b64_tr_b16 v[98:99], %[ha] offset:6976\n\t" \
  "v_mfma_f32_16x16x32_bf16 v[140:143], v[88:91], v[104:107], v[140:143]\n\t" \
  "s_waitcnt vmcnt(0)\n\t" \
  "s_mov_b64 exec, 1\n\t" \
  "global_store_dwordx2 %[fo], %[tg], %[wsb] sc1\n\t" \
  "s_mov_b64 exec, -1\n\t" \
  "v_mfma_f32_16x16x32_bf16 v[144:147], v[112:115], v[104:107], v[144:147]\n\t" \
  "v_mfma_f32_16x16x32_bf16 v[148:151], v[40:43], v[104:107], v[148:151]\n\t" \
  "v_mfma_f32_16x16x32_bf16 v[152:155], v[72:75], v[104:107], v[152:155]\n\t" \
  "s_waitcnt lgkmcnt(15)\n\t" \
  "v_mfma_f32_16x16x32_bf16 v[156:159], v[108:111], v[104:107], v[156:159]\n\t" \
  "v_mfma_f32_16x16x32_bf16 v[164:167], v[36:39], v[116:119], v[164:167]\n\t" \
  "ds_read_b64_tr_b16 v[104:105], %[ha] offset:6432\n\t" \
  "ds_read_b64_tr_b16 v[106:107], %[ha] offset:6464\n\t" \
  "v_mfma_f32_16x16x32_bf16 v[168:171], v[64:67], v[116:119], v[168:171]\n\t" \
  "v_mfma_f32_16x16x32_bf16 v[172:175], v[88:91], v[116:119], v[172:175]\n\t" \
  "v_mfma_f32_16x16x32_bf16 v[176:179], v[112:115], v[116:119], v[176:179]\n\t" \
  "v_mfma_f32_16x16x32_bf16 v[180:183], v[40:43], v[116:119], v[180:183]\n\t" \
  "v_mfma_f32_16x16x32_bf16 v[184:187], v[72:75], v[116:119], v[184:187]\n\t" \
  "v_mfma_f32_16x16x32_bf16 v[188:191], v[108:111], v[116:119], v[188:191]\n\t" \
  "v_mfma_f32_16x16x32_bf16 v[128:131], v[24:27], v[120:123], 0\n\t" \
  "ds_read_b64_tr_b16 v[116:117], %[ha] offset:5920\n\t" \
  "ds_read_b64_tr_b16 v[118:119], %[ha] offset:5952\n\t" \
  "v_mfma_f32_16x16x32_bf16 v[132:135], v[52:55], v[120:123], v[132:135]\n\t" \
  "v_mfma_f32_16x16x32_bf16 v[136:139], v[76:79], v[120:123], v[136:139]\n\t" \
  "v_mfma_f32_16x16x32_bf16 v[140:143], v[100:103], v[120:123], v[140:143]\n\t" \
  "v_mfma_f32_16x16x32_bf16 v[144:147], v[124:127], v[120:123], v[144:147]\n\t" \
  "v_mfma_f32_16x16x32_bf16 v[148:151], v[56:59], v[120:123], v[148:151]\n\t" \
  "v_mfma_f32_16x16x32_bf16 v[152:155], v[92:95], v[120:123], v[152:155]\n\t" \
  "v_mfma_f32_16x16x32_bf16 v[156:159], v[28:31], v[120:123], v[156:159]\n\t" \
  "s_waitcnt lgkmcnt(15)\n\t" \
  "v_mfma_f32_16x16x32_bf16 v[160:163], v[24:27], v[32:35], 0\n\t" \
  "ds_read_b64_tr_b16 v[120:121], %[ha] offset:5408\n\t" \
  "v_mfma_f32_16x16x32_bf16 v[164:167], v[52:55], v[32:35], v[164:167]\n\t" \
  "ds_read_b64_tr_b16 v[122:123], %[ha] offset:5440\n\t" \
  "ds_read_b64_tr_b16 v[24:25], %[ha] offset:4896\n\t" \
  "v_mfma_f32_16x16x32_bf16 v[168:171], v[76:79], v[32:35], v[168:171]\n\t" \
  "ds_read_b64_tr_b16 v[26:27], %[ha] offset:4928\n\t" \
  "ds_read_b64_tr_b16 v[52:53], %[ha] offset:4384\n\t" \
  "v_mfma_f32_16x16x32_bf16 v[172:175], v[100:103], v[32:35], v[172:175]\n\t" \
  "ds_read_b64_tr_b16 v[54:55], %[ha] offset:4416\n\t" \
  "ds_read_b64 v[76:77], %[xa] offset:14976\n\t" \
  "v_mfma_f32_16x16x32_bf16 v[176:179], v[124:127], v[32:35], v[176:179]\n\t" \
  "ds_read_b64 v[78:79], %[xa] offset:14984\n\t" \
  "ds_read_b64_tr_b16 v[100:101], %[ha] offset:7712\n\t" \
  "v_mfma_f32_16x16x32_bf16 v[180:183], v[56:59], v[32:35], v[180:183]\n\t" \
  "ds_read_b64_tr_b16 v[102:103], %[ha] offset:7744\n\t" \
  "ds_read_b64 v[124:125], %[xa] offset:10240\n\t" \
  "v_mfma_f32_16x16x32_bf16 v[184:187], v[92:95], v[32:35], v[184:187]\n\t" \
  "ds_read_b64 v[126:127], %[xa] offset:10248\n\t" \
  "ds_read_b64_tr_b16 v[56:57], %[ha] offset:7200\n\t" \
  "v_mfma_f32_16x16x32_bf16 v[188:191], v[28:31], v[32:35], v[188:191]\n\t" \
  "ds_read_b64_tr_b16 v[58:59], %[ha] offset:7232\n\t" \
  "ds_read_b64_tr_b16 v[92:93], %[ha] offset:6688\n\t" \
  "v_mfma_f32_16x16x32_bf16 v[128:131], v[36:39], v[44:47], v[128:131]\n\t" \
  "v_mfma_f32_16x16x32_bf16 v[132:135], v[64:67], v[44:47], v[132:135]\n\t" \
  "v_mfma_f32_16x16x32_bf16 v[136:139], v[88:91], v[44:47], v[136:139]\n\t" \
  "ds_read_b64_tr_b16 v[94:95], %[ha] offset:6720\n\t" \
  "v_mfma_f32_16x16x32_bf16 v[140:143], v[112:115], v[44:47], v[140:143]\n\t" \
  "ds_read_b64_tr_b16 v[28:29], %[ha] offset:6176\n\t" \
  "ds_read_b64_tr_b16 v[30:31], %[ha] offset:6208\n\t" \
  "v_mfma_f32_16x16x32_bf16 v[144:147], v[40:43], v[44:47], v[144:147]\n\t" \
  "ds_read_b64_tr_b16 v[32:33], %[ha] offset:5664\n\t" \
  "ds_read_b64_tr_b16 v[34:35], %[ha] offset:5696\n\t" \
  "v_mfma_f32_16x16x32_bf16 v[148:151], v[72:75], v[44:47], v[148:151]\n\t" \
  "v_mfma_f32_16x16x32_bf16 v[152:155], v[108:111], v[44:47], v[152:155]\n\t" \
  "s_waitcnt lgkmcnt(15)\n\t" \
  "v_mfma_f32_16x16x32_bf16 v[156:159], v[48:51], v[44:47], v[156:159]\n\t" \
  "v_mfma_f32_16x16x32_bf16 v[160:163], v[36:39], v[60:63], v[160:163]\n\t" \
  "ds_read_b64_tr_b16 v[44:45], %[ha] offset:5152\n\t" \
  "ds_read_b64_tr_b16 v[46:47], %[ha] offset:5184\n\t" \
  "v_mfma_f32_16x16x32_bf16 v[164:167], v[64:67], v[60:63], v[164:167]\n\t" \
  "ds_read_b64_tr_b16 v[36:37], %[ha] offset:4640\n\t" \
  "ds_read_b64_tr_b16 v[38:39], %[ha] offset:4672\n\t" \
  "v_mfma_f32_16x16x32_bf16 v[168:171], v[88:91], v[60:63], v[168:171]\n\t" \
  "ds_read_b64_tr_b16 v[64:65], %[ha] offset:4128\n\t" \
  "ds_read_b64_tr_b16 v[66:67], %[ha] offset:4160\n\t" \
  "v_mfma_f32_16x16x32_bf16 v[172:175], v[112:115], v[60:63], v[172:175]\n\t" \
  "ds_read_b64 v[88:89], %[xa] offset:14912\n\t" \
  "ds_read_b64 v[90:91], %[xa] offset:14920\n\t" \
  "v_mfma_f32_16x16x32_bf16 v[176:179], v[40:43], v[60:63], v[176:179]\n\t" \
  "ds_read_b64 v[112:113], %[xa] offset:10176\n\t" \
  "ds_read_b64 v[114:115], %[xa] offset:10184\n\t" \
  "v_mfma_f32_16x16x32_bf16 v[180:183], v[72:75], v[60:63], v[180:183]\n\t" \
  "ds_read_b64 v[40:41], %[xa] offset:14848\n\t" \
  "ds_read_b64 v[42:43], %[xa] offset:14856\n\t" \
  "v_mfma_f32_16x16x32_bf16 v[184:187], v[108:111], v[60:63], v[184:187]\n\t" \
  "ds_read_b64 v[72:73], %[xa] offset:10112\n\t" \
  "ds_read_b64 v[74:75], %[xa] offset:10120\n\t" \
  "v_mfma_f32_16x16x32_bf16 v[188:191], v[48:51], v[60:63], v[188:191]\n\t" \
  "v_mfma_f32_16x16x32_bf16 v[156:159], v[68:71], v[80:83], v[156:159]\n\t" \
  "v_mfma_f32_16x16x32_bf16 v[152:155], v[84:87], v[80:83], v[152:155]\n\t" \
  "v_mfma_f32_16x16x32_bf16 v[148:151], v[96:99], v[80:83], v[148:151]\n\t" \
  "v_mfma_f32_16x16x32_bf16 v[144:147], v[104:107], v[80:83], v[144:147]\n\t" \
  "v_mfma_f32_16x16x32_bf16 v[140:143], v[116:119], v[80:83], v[140:143]\n\t" \
  "v_mfma_f32_16x16x32_bf16 v[136:139], v[120:123], v[80:83], v[136:139]\n\t" \
  "ds_read_b64 v[108:109], %[xa] offset:14784\n\t" \
  "ds_read_b64 v[110:111], %[xa] offset:14792\n\t" \
  "v_mfma_f32_16x16x32_bf16 v[132:135], v[24:27], v[80:83], v[132:135]\n\t" \
  "s_waitcnt lgkmcnt(15)\n\t" \
  "v_mfma_f32_16x16x32_bf16 v[128:131], v[52:55], v[80:83], v[128:131]\n\t" \
  "v_mfma_f32_16x16x32_bf16 v[188:191], v[68:71], v[76:79], v[188:191]\n\t" \
  "v_mfma_f32_16x16x32_bf16 v[184:187], v[84:87], v[76:79], v[184:187]\n\t" \
  "v_mfma_f32_16x16x32_bf16 v[180:183], v[96:99], v[76:79], v[180:183]\n\t" \
  "v_mfma_f32_16x16x32_bf16 v[176:179], v[104:107], v[76:79], v[176:179]\n\t" \
  "v_mfma_f32_16x16x32_bf16 v[172:175], v[116:119], v[76:79], v[172:175]\n\t" \
  "ds_read_b64 v[48:49], %[xa] offset:10048\n\t" \
  "ds_read_b64 v[50:51], %[xa] offset:10056\n\t" \
  "v_mfma_f32_16x16x32_bf16 v[168:171], v[120:123], v[76:79], v[168:171]\n\t" \
  "v_mfma_f32_16x16x32_bf16 v[164:167], v[24:27], v[76:79], v[164:167]\n\t" \
  "v_mfma_f32_16x16x32_bf16 v[160:163], v[52:55], v[76:79], v[160:163]\n\t" \
  "v_mfma_f32_16x16x32_bf16 v[156:159], v[100:103], v[124:127], v[156:159]\n\t" \
  "v_mfma_f32_16x16x32_bf16 v[152:155], v[56:59], v[124:127], v[152:155]\n\t" \
  "v_mfma_f32_16x16x32_bf16 v[148:151], v[92:95], v[124:127], v[148:151]\n\t" \
  "ds_read_b64 v[60:61], %[xa] offset:14720\n\t" \
  "ds_read_b64 v[62:63], %[xa] offset:14728\n\t" \
  "v_mfma_f32_16x16x32_bf16 v[144:147], v[28:31], v[124:127], v[144:147]\n\t" \
  "v_mfma_f32_16x16x32_bf16 v[140:143], v[32:35], v[124:127], v[140:143]\n\t" \
  "s_waitcnt lgkmcnt(15)\n\t" \
  "v_mfma_f32_16x16x32_bf16 v[136:139], v[44:47], v[124:127], v[136:139]\n\t" \
  "v_mfma_f32_16x16x32_bf16 v[132:135], v[36:39], v[124:127], v[132:135]\n\t" \
  "s_waitcnt lgkmcnt(14)\n\t" \
  "v_mfma_f32_16x16x32_bf16 v[128:131], v[64:67], v[124:127], v[128:131]\n\t" \
  "s_waitcnt lgkmcnt(12)\n\t" \
  "v_mfma_f32_16x16x32_bf16 v[188:191], v[100:103], v[88:91], v[188:191]\n\t" \
  "ds_read_b64 v[80:81], %[xa] offset:9984\n\t" \
  "v_mfma_f32_16x16x32_bf16 v[184:187], v[56:59], v[88:91], v[184:187]\n\t" \
  "ds_read_b64 v[82:83], %[xa] offset:9992\n\t" \
  "v_mfma_f32_16x16x32_bf16 v[180:183], v[92:95], v[88:91], v[180:183]\n\t" \
  "v_mfma_f32_16x16x32_bf16 v[176:179], v[28:31], v[88:91], v[176:179]\n\t" \
  "v_mfma_f32_16x16x32_bf16 v[172:175], v[32:35], v[88:91], v[172:175]\n\t" \
  "v_mfma_f32_16x16x32_bf16 v[168:171], v[44:47], v[88:91], v[168:171]\n\t" \
  "v_mfma_f32_16x16x32_bf16 v[164:167], v[36:39], v[88:91], v[164:167]\n\t" \
  "ds_read_b64 v[68:69], %[xa] offset:14656\n\t" \
  "ds_read_b64 v[70:71], %[xa] offset:14664\n\t" \
  "v_mfma_f32_16x16x32_bf16 v[160:163], v[64:67], v[88:91], v[160:163]\n\t" \
  "s_waitcnt lgkmcnt(14)\n\t" \
  "v_mfma_f32_16x16x32_bf16 v[156:159], v[84:87], v[112:115], v[156:159]\n\t" \
  "v_mfma_f32_16x16x32_bf16 v[152:155], v[96:99], v[112:115], v[152:155]\n\t" \
  "v_mfma_f32_16x16x32_bf16 v[148:151], v[104:107], v[112:115], v[148:151]\n\t" \
  "v_mfma_f32_16x16x32_bf16 v[144:147], v[116:119], v[112:115], v[144:147]\n\t" \
  "v_mfma_f32_16x16x32_bf16 v[140:143], v[120:123], v[112:115], v[140:143]\n\t" \
  "ds_read_b64 v[76:77], %[xa] offset:9920\n\t" \
  "ds_read_b64 v[78:79], %[xa] offset:9928\n\t" \
  "v_mfma_f32_16x16x32_bf16 v[136:139], v[24:27], v[112:115], v[136:139]\n\t" \
  "v_mfma_f32_16x16x32_bf16 v[132:135], v[52:55], v[112:115], v[132:135]\n\t" \
  "s_waitcnt lgkmcnt(14)\n\t" \
  "v_mfma_f32_16x16x32_bf16 v[188:191], v[84:87], v[40:43], v[188:191]\n\t" \
  "ds_bpermute_b32 v192, %[sl], v163\n\t" \
  "v_mfma_f32_16x16x32_bf16 v[184:187], v[96:99], v[40:43], v[184:187]\n\t" \
  "v_mfma_f32_16x16x32_bf16 v[180:183], v[104:107], v[40:43], v[180:183]\n\t" \
  "ds_read_b64 v[124:125], %[xa] offset:14592\n\t" \
  "ds_read_b64 v[126:127], %[xa] offset:14600\n\t" \
  "v_mfma_f32_16x16x32_bf16 v[176:179], v[116:119], v[40:43], v[176:179]\n\t" \
  "v_mfma_f32_16x16x32_bf16 v[172:175], v[120:123], v[40:43], v[172:175]\n\t" \
  "v_mfma_f32_16x16x32_bf16 v[168:171], v[24:27], v[40:43], v[168:171]\n\t" \
  "v_mfma_f32_16x16x32_bf16 v[164:167], v[52:55], v[40:43], v[164:167]\n\t" \
  "v_add_f32 v129, v129, v160\n\t" \
  "v_add_f32 v130, v130, v161\n\t" \
  "s_waitcnt lgkmcnt(15)\n\t" \
  "v_mfma_f32_16x16x32_bf16 v[156:159], v[56:59], v[72:75], v[156:159]\n\t" \
  "v_add_f32 v131, v131, v162\n\t" \
  "s_waitcnt lgkmcnt(2)\n\t" \
  "v_mfma_f32_16x16x32_bf16 v[152:155], v[92:95], v[72:75], v[152:155]\n\t" \
  "v_cndmask_b32 v163, v192, 0, %[l0]\n\t" \
  "v_cvt_pk_bf16_f32 v201, v130, v131\n\t" \
  "v_mfma_f32_16x16x32_bf16 v[148:151], v[28:31], v[72:75], v[148:151]\n\t" \
  "v_add_f32 v128, v128, v163\n\t" \
  "v_cvt_pk_bf16_f32 v200, v128, v129\n\t" \
  "v_mfma_f32_16x16x32_bf16 v[144:147], v[32:35], v[72:75], v[144:147]\n\t" \
  "ds_read_b64 v[100:101], %[xa] offset:9856\n\t" \
  "ds_read_b64 v[102:103], %[xa] offset:9864\n\t" \
  "v_mfma_f32_16x16x32_bf16 v[140:143], v[44:47], v[72:75], v[140:143]\n\t" \
  "v_mfma_f32_16x16x32_bf16 v[136:139], v[36:39], v[72:75], v[136:139]\n\t" \
  "ds_read_b64 v[88:89], %[xa] offset:14528\n\t" \
  "ds_read_b64 v[90:91], %[xa] offset:14536\n\t" \
  "v_mfma_f32_16x16x32_bf16 v[132:135], v[64:67], v[72:75], v[132:135]\n\t" \
  "v_mfma_f32_16x16x32_bf16 v[188:191], v[56:59], v[108:111], v[188:191]\n\t" \
  "v_mfma_f32_16x16x32_bf16 v[184:187], v[92:95], v[108:111], v[184:187]\n\t" \
  "v_mfma_f32_16x16x32_bf16 v[180:183], v[28:31], v[108:111], v[180:183]\n\t" \
  "v_mfma_f32_16x16x32_bf16 v[176:179], v[32:35], v[108:111], v[176:179]\n\t" \
  "ds_read_b64 v[112:113], %[xa] offset:14464\n\t" \
  "ds_read_b64 v[114:115], %[xa] offset:14472\n\t" \
  "v_mfma_f32_16x16x32_bf16 v[172:175], v[44:47], v[108:111], v[172:175]\n\t" \
  "ds_read_b64 v[84:85], %[xa] offset:14400\n\t" \
  "ds_read_b64 v[86:87], %[xa] offset:14408\n\t" \
  "v_mfma_f32_16x16x32_bf16 v[168:171], v[36:39], v[108:111], v[168:171]\n\t" \
  "ds_read_b64 v[40:41], %[xa] offset:9792\n\t" \
  "ds_read_b64 v[42:43], %[xa] offset:9800\n\t" \
  "v_mfma_f32_16x16x32_bf16 v[164:167], v[64:67], v[108:111], v[164:167]\n\t" \
  "ds_read_b64 v[72:73], %[xa] offset:9728\n\t" \
  "ds_read_b64 v[74:75], %[xa] offset:9736\n\t" \
  "v_mfma_f32_16x16x32_bf16 v[156:159], v[96:99], v[48:51], v[156:159]\n\t" \
  "v_mfma_f32_16x16x32_bf16 v[152:155], v[104:107], v[48:51], v[152:155]\n\t" \
  "v_mfma_f32_16x16x32_bf16 v[148:151], v[116:119], v[48:51], v[148:151]\n\t" \
  "ds_read_b64 v[56:57], %[xa] offset:14336\n\t" \
  "ds_read_b64 v[58:59], %[xa] offset:14344\n\t" \
  "v_mfma_f32_16x16x32_bf16 v[144:147], v[120:123], v[48:51], v[144:147]\n\t" \
  "ds_read_b64 v[108:109], %[xa] offset:14272\n\t" \
  "ds_read_b64 v[110:111], %[xa] offset:14280\n\t" \
  "v_mfma_f32_16x16x32_bf16 v[140:143], v[24:27], v[48:51], v[140:143]\n\t" \
  "v_mfma_f32_16x16x32_bf16 v[136:139], v[52:55], v[48:51], v[136:139]\n\t" \
  "v_mfma_f32_16x16x32_bf16 v[188:191], v[96:99], v[60:63], v[188:191]\n\t" \
  "ds_read_b64 v[48:49], %[xa] offset:9664\n\t" \
  "ds_read_b64 v[50:51], %[xa] offset:9672\n\t" \
  "v_mfma_f32_16x16x32_bf16 v[184:187], v[104:107], v[60:63], v[184:187]\n\t" \
  "ds_read_b64 v[96:97], %[xa] offset:9600\n\t" \
  "ds_bpermute_b32 v193, %[sl], v167\n\t" \
  "v_mfma_f32_16x16x32_bf16 v[180:183], v[116:119], v[60:63], v[180:183]\n\t" \
  "ds_read_b64 v[98:99], %[xa] offset:9608\n\t" \
  "v_mfma_f32_16x16x32_bf16 v[176:179], v[120:123], v[60:63], v[176:179]\n\t" \
  "v_mfma_f32_16x16x32_bf16 v[172:175], v[24:27], v[60:63], v[172:175]\n\t" \
  "v_mfma_f32_16x16x32_bf16 v[168:171], v[52:55], v[60:63], v[168:171]\n\t" \
  "v_mfma_f32_16x16x32_bf16 v[156:159], v[92:95], v[80:83], v[156:159]\n\t" \
  "ds_read_b64 v[60:61], %[xa] offset:14208\n\t" \
  "ds_read_b64 v[62:63], %[xa] offset:14216\n\t" \
  "v_mfma_f32_16x16x32_bf16 v[152:155], v[28:31], v[80:83], v[152:155]\n\t" \
  "v_add_f32 v133, v133, v164\n\t" \
  "v_add_f32 v134, v134, v165\n\t" \
  "v_mfma_f32_16x16x32_bf16 v[148:151], v[32:35], v[80:83], v[148:151]\n\t" \
  "v_add_f32 v135, v135, v166\n\t" \
  "s_waitcnt lgkmcnt(3)\n\t" \
  "v_mfma_f32_16x16x32_bf16 v[144:147], v[44:47], v[80:83], v[144:147]\n\t" \
  "v_cndmask_b32 v167, v193, v192, %[l0]\n\t" \
  "v_cvt_pk_bf16_f32 v203, v134, v135\n\t" \
  "v_mfma_f32_16x16x32_bf16 v[140:143], v[36:39], v[80:83], v[140:143]\n\t" \
  "v_add_f32 v132, v132, v167\n\t" \
  "v_cvt_pk_bf16_f32 v202, v132, v133\n\t" \
  "v_mfma_f32_16x16x32_bf16 v[136:139], v[64:67], v[80:83], v[136:139]\n\t" \
  "v_mfma_f32_16x16x32_bf16 v[188:191], v[92:95], v[68:71], v[188:191]\n\t" \
  "v_permlane16_swap_b32 v200, v202\n\t" \
  "v_permlane16_swap_b32 v201, v203\n\t" \
  "v_mfma_f32_16x16x32_bf16 v[184:187], v[28:31], v[68:71], v[184:187]\n\t" \
  "ds_bpermute_b32 v200, %[s2], v200\n\t" \
  "ds_bpermute_b32 v201, %[s2], v201\n\t" \
  "v_mfma_f32_16x16x32_bf16 v[180:183], v[32:35], v[68:71], v[180:183]\n\t" \
  "ds_bpermute_b32 v202, %[s2], v202\n\t" \
  "ds_bpermute_b32 v203, %[s2], v203\n\t" \
  "v_mfma_f32_16x16x32_bf16 v[176:179], v[44:47], v[68:71], v[176:179]\n\t" \
  "ds_read_b64 v[80:81], %[xa] offset:14144\n\t" \
  "ds_read_b64 v[82:83], %[xa] offset:14152\n\t" \
  "v_mfma_f32_16x16x32_bf16 v[172:175], v[36:39], v[68:71], v[172:175]\n\t" \
  "ds_read_b64 v[92:93], %[xa] offset:9536\n\t" \
  "ds_read_b64 v[94:95], %[xa] offset:9544\n\t" \
  "v_mfma_f32_16x16x32_bf16 v[168:171], v[64:67], v[68:71], v[168:171]\n\t" \
  "v_mfma_f32_16x16x32_bf16 v[156:159], v[104:107], v[76:79], v[156:159]\n\t" \
  "ds_read_b64 v[68:69], %[xa] offset:9472\n\t" \
  "ds_read_b64 v[70:71], %[xa] offset:9480\n\t" \
  "v_mfma_f32_16x16x32_bf16 v[152:155], v[116:119], v[76:79], v[152:155]\n\t" \
  "v_mfma_f32_16x16x32_bf16 v[148:151], v[120:123], v[76:79], v[148:151]\n\t" \
  "v_mfma_f32_16x16x32_bf16 v[144:147], v[24:27], v[76:79], v[144:147]\n\t" \
  "v_mfma_f32_16x16x32_bf16 v[140:143], v[52:55], v[76:79], v[140:143]\n\t" \
  "v_mfma_f32_16x16x32_bf16 v[188:191], v[104:107], v[124:127], v[188:191]\n\t" \
  "ds_read_b64 v[76:77], %[xa] offset:14080\n\t" \
  "ds_read_b64 v[78:79], %[xa] offset:14088\n\t" \
  "v_mfma_f32_16x16x32_bf16 v[184:187], v[116:119], v[124:127], v[184:187]\n\t" \
  "s_waitcnt lgkmcnt(8)\n\t" \
  "buffer_store_dwordx4 v[200:203], %[yo], %[rs], 0 offen sc1\n\t" \
  "v_add_u32 %[yo], 0x10000, %[yo]\n\t" \
  "v_mfma_f32_16x16x32_bf16 v[180:183], v[120:123], v[124:127], v[180:183]\n\t" \
  "ds_read_b64 v[104:105], %[xa] offset:14016\n\t" \
  "ds_bpermute_b32 v194, %[sl], v171\n\t" \
  "v_mfma_f32_16x16x32_bf16 v[176:179], v[24:27], v[124:127], v[176:179]\n\t" \
  "ds_read_b64 v[106:107], %[xa] offset:14024\n\t" \
  "v_mfma_f32_16x16x32_bf16 v[172:175], v[52:55], v[124:127], v[172:175]\n\t" \
  "v_mfma_f32_16x16x32_bf16 v[156:159], v[28:31], v[100:103], v[156:159]\n\t" \
  "ds_read_b64 v[124:125], %[xa] offset:9408\n\t" \
  "ds_read_b64 v[126:127], %[xa] offset:9416\n\t" \
  "v_mfma_f32_16x16x32_bf16 v[152:155], v[32:35], v[100:103], v[152:155]\n\t" \
  "v_mfma_f32_16x16x32_bf16 v[148:151], v[44:47], v[100:103], v[148:151]\n\t" \
  "v_mfma_f32_16x16x32_bf16 v[144:147], v[36:39], v[100:103], v[144:147]\n\t" \
  "v_add_f32 v137, v137, v168\n\t" \
  "v_add_f32 v138, v138, v169\n\t" \
  "v_mfma_f32_16x16x32_bf16 v[140:143], v[64:67], v[100:103], v[140:143]\n\t" \
  "v_add_f32 v139, v139, v170\n\t" \
  "s_waitcnt lgkmcnt(3)\n\t" \
  "v_mfma_f32_16x16x32_bf16 v[188:191], v[28:31], v[88:91], v[188:191]\n\t" \
  "v_cndmask_b32 v171, v194, v193, %[l0]\n\t" \
  "v_cvt_pk_bf16_f32 v205, v138, v139\n\t" \
  "v_mfma_f32_16x16x32_bf16 v[184:187], v[32:35], v[88:91], v[184:187]\n\t" \
  "v_add_f32 v136, v136, v171\n\t" \
  "v_cvt_pk_bf16_f32 v204, v136, v137\n\t" \
  "v_mfma_f32_16x16x32_bf16 v[180:183], v[44:47], v[88:91], v[180:183]\n\t" \
  "ds_read_b64 v[100:101], %[xa] offset:9344\n\t" \
  "ds_read_b64 v[102:103], %[xa] offset:9352\n\t" \
  "v_mfma_f32_16x16x32_bf16 v[176:179], v[36:39], v[88:91], v[176:179]\n\t" \
  "v_mfma_f32_16x16x32_bf16 v[172:175], v[64:67], v[88:91], v[172:175]\n\t" \
  "v_mfma_f32_16x16x32_bf16 v[176:179], v[52:55], v[112:115], v[176:179]\n\t" \
  "v_mfma_f32_16x16x32_bf16 v[176:179], v[64:67], v[84:87], v[176:179]\n\t" \
  "v_mfma_f32_16x16x32_bf16 v[144:147], v[52:55], v[40:43], v[144:147]\n\t" \
  "v_mfma_f32_16x16x32_bf16 v[144:147], v[64:67], v[72:75], v[144:147]\n\t" \
  "v_mfma_f32_16x16x32_bf16 v[180:183], v[24:27], v[112:115], v[180:183]\n\t" \
  "v_mfma_f32_16x16x32_bf16 v[180:183], v[36:39], v[84:87], v[180:183]\n\t" \
  "v_mfma_f32_16x16x32_bf16 v[180:183], v[52:55], v[56:59], v[180:183]\n\t" \
  "v_mfma_f32_16x16x32_bf16 v[180:183], v[64:67], v[108:111], v[180:183]\n\t" \
  "ds_bpermute_b32 v195, %[sl], v175\n\t" \
  "v_mfma_f32_16x16x32_bf16 v[148:151], v[24:27], v[40:43], v[148:151]\n\t" \
  "v_mfma_f32_16x16x32_bf16 v[148:151], v[36:39], v[72:75], v[148:151]\n\t" \
  "ds_bpermute_b32 v196, %[sl], v179\n\t" \
  "v_mfma_f32_16x16x32_bf16 v[148:151], v[52:55], v[48:51], v[148:151]\n\t" \
  "v_mfma_f32_16x16x32_bf16 v[148:151], v[64:67], v[96:99], v[148:151]\n\t" \
  "v_mfma_f32_16x16x32_bf16 v[184:187], v[120:123], v[112:115], v[184:187]\n\t" \
  "v_mfma_f32_16x16x32_bf16 v[184:187], v[44:47], v[84:87], v[184:187]\n\t" \
  "v_add_f32 v141, v141, v172\n\t" \
  "v_add_f32 v142, v142, v173\n\t" \
  "v_mfma_f32_16x16x32_bf16 v[184:187], v[24:27], v[56:59], v[184:187]\n\t" \
  "v_add_f32 v143, v143, v174\n\t" \
  "s_waitcnt lgkmcnt(1)\n\t" \
  "v_mfma_f32_16x16x32_bf16 v[184:187], v[36:39], v[108:111], v[184:187]\n\t" \
  "v_cndmask_b32 v175, v195, v194, %[l0]\n\t" \
  "v_cvt_pk_bf16_f32 v207, v142, v143\n\t" \
  "ds_bpermute_b32 v197, %[sl], v183\n\t" \
  "v_mfma_f32_16x16x32_bf16 v[184:187], v[52:55], v[60:63], v[184:187]\n\t" \
  "v_add_f32 v140, v140, v175\n\t" \
  "v_cvt_pk_bf16_f32 v206, v140, v141\n\t" \
  "v_mfma_f32_16x16x32_bf16 v[184:187], v[64:67], v[80:83], v[184:187]\n\t" \
  "v_mfma_f32_16x16x32_bf16 v[152:155], v[120:123], v[40:43], v[152:155]\n\t" \
  "v_permlane16_swap_b32 v204, v206\n\t" \
  "v_permlane16_swap_b32 v205, v207\n\t" \
  "v_mfma_f32_16x16x32_bf16 v[152:155], v[44:47], v[72:75], v[152:155]\n\t" \
  "ds_bpermute_b32 v204, %[s2], v204\n\t" \
  "ds_bpermute_b32 v205, %[s2], v205\n\t" \
  "v_mfma_f32_16x16x32_bf16 v[152:155], v[24:27], v[48:51], v[152:155]\n\t" \
  "ds_bpermute_b32 v206, %[s2], v206\n\t" \
  "ds_bpermute_b32 v207, %[s2], v207\n\t" \
  "v_mfma_f32_16x16x32_bf16 v[152:155], v[36:39], v[96:99], v[152:155]\n\t" \
  "v_add_f32 v145, v145, v176\n\t" \
  "v_add_f32 v146, v146, v177\n\t" \
  "v_mfma_f32_16x16x32_bf16 v[152:155], v[52:55], v[92:95], v[152:155]\n\t" \
  "v_add_f32 v147, v147, v178\n\t" \
  "s_waitcnt lgkmcnt(5)\n\t" \
  "v_mfma_f32_16x16x32_bf16 v[152:155], v[64:67], v[68:71], v[152:155]\n\t" \
  "v_cndmask_b32 v179, v196, v195, %[l0]\n\t" \
  "v_cvt_pk_bf16_f32 v209, v146, v147\n\t" \
  "v_mfma_f32_16x16x32_bf16 v[188:191], v[116:119], v[112:115], v[188:191]\n\t" \
  "v_add_f32 v144, v144, v179\n\t" \
  "v_cvt_pk_bf16_f32 v208, v144, v145\n\t" \
  "v_mfma_f32_16x16x32_bf16 v[188:191], v[32:35], v[84:87], v[188:191]\n\t" \
  "v_add_f32 v149, v149, v180\n\t" \
  "v_add_f32 v150, v150, v181\n\t" \
  "ds_bpermute_b32 v198, %[sl], v187\n\t" \
  "v_mfma_f32_16x16x32_bf16 v[188:191], v[120:123], v[56:59], v[188:191]\n\t" \
  "v_add_f32 v151, v151, v182\n\t" \
  "s_waitcnt lgkmcnt(5)\n\t" \
  "v_mfma_f32_16x16x32_bf16 v[188:191], v[44:47], v[108:111], v[188:191]\n\t" \
  "v_cndmask_b32 v183, v197, v196, %[l0]\n\t" \
  "v_cvt_pk_bf16_f32 v211, v150, v151\n\t" \
  "v_mfma_f32_16x16x32_bf16 v[188:191], v[24:27], v[60:63], v[188:191]\n\t" \
  "v_add_f32 v148, v148, v183\n\t" \
  "v_cvt_pk_bf16_f32 v210, v148, v149\n\t" \
  "v_mfma_f32_16x16x32_bf16 v[188:191], v[36:39], v[80:83], v[188:191]\n\t" \
  "v_mfma_f32_16x16x32_bf16 v[188:191], v[52:55], v[76:79], v[188:191]\n\t" \
  "s_waitcnt lgkmcnt(1)\n\t" \
  "buffer_store_dwordx4 v[204:207], %[yo], %[rs], 0 offen sc1\n\t" \
  "v_add_u32 %[yo], 0x10000, %[yo]\n\t" \
  "v_permlane16_swap_b32 v208, v210\n\t" \
  "v_permlane16_swap_b32 v209, v211\n\t" \
  "v_mfma_f32_16x16x32_bf16 v[188:191], v[64:67], v[104:107], v[188:191]\n\t" \
  "ds_bpermute_b32 v208, %[s2], v208\n\t" \
  "ds_bpermute_b32 v209, %[s2], v209\n\t" \
  "v_mfma_f32_16x16x32_bf16 v[156:159], v[116:119], v[40:43], v[156:159]\n\t" \
  "ds_bpermute_b32 v210, %[s2], v210\n\t" \
  "ds_bpermute_b32 v211, %[s2], v211\n\t" \
  "v_mfma_f32_16x16x32_bf16 v[156:159], v[32:35], v[72:75], v[156:159]\n\t" \
  "v_add_f32 v153, v153, v184\n\t" \
  "v_add_f32 v154, v154, v185\n\t" \
  "v_mfma_f32_16x16x32_bf16 v[156:159], v[120:123], v[48:51], v[156:159]\n\t" \
  "v_add_f32 v155, v155, v186\n\t" \
  "s_waitcnt lgkmcnt(4)\n\t" \
  "v_mfma_f32_16x16x32_bf16 v[156:159], v[44:47], v[96:99], v[156:159]\n\t" \
  "v_cndmask_b32 v187, v198, v197, %[l0]\n\t" \
  "v_cvt_pk_bf16_f32 v213, v154, v155\n\t" \
  "ds_bpermute_b32 v199, %[sl], v191\n\t" \
  "v_mfma_f32_16x16x32_bf16 v[156:159], v[24:27], v[92:95], v[156:159]\n\t" \
  "v_add_f32 v152, v152, v187\n\t" \
  "v_cvt_pk_bf16_f32 v212, v152, v153\n\t" \
  "v_mfma_f32_16x16x32_bf16 v[156:159], v[36:39], v[68:71], v[156:159]\n\t" \
  "v_mfma_f32_16x16x32_bf16 v[156:159], v[52:55], v[124:127], v[156:159]\n\t" \
  "v_mfma_f32_16x16x32_bf16 v[156:159], v[64:67], v[100:103], v[156:159]\n\t" \
  "s_barrier\n\t" \
  "s_waitcnt lgkmcnt(1)\n\t" \
  "buffer_store_dwordx4 v[208:211], %[yo], %[rs], 0 offen sc1\n\t" \
  "v_add_u32 %[yo], 0x10000, %[yo]\n\t" \
  "s_nop 6\n\t" \
  "v_add_f32 v157, v157, v188\n\t" \
  "v_add_f32 v158, v158, v189\n\t" \
  "v_add_f32 v159, v159, v190\n\t" \
  "s_waitcnt lgkmcnt(0)\n\t" \
  "v_cndmask_b32 v191, v199, v198, %[l0]\n\t" \
  "v_cvt_pk_bf16_f32 v215, v158, v159\n\t" \
  "v_add_f32 v156, v156, v191\n\t" \
  "v_cvt_pk_bf16_f32 v214, v156, v157\n\t" \
  "s_nop 1\n\t" \
  "v_permlane16_swap_b32 v212, v214\n\t" \
  "v_permlane16_swap_b32 v213, v215\n\t" \
  "ds_bpermute_b32 v212, %[s2], v212\n\t" \
  "ds_bpermute_b32 v213, %[s2], v213\n\t" \
  "ds_bpermute_b32 v214, %[s2], v214\n\t" \
  "ds_bpermute_b32 v215, %[s2], v215\n\t" \
  "s_waitcnt lgkmcnt(0)\n\t" \
  "buffer_store_dwordx4 v[212:215], %[yo], %[rs], 0 offen sc1\n\t" \
  "v_add_u32 %[yo], 0x10000, %[yo]\n\t" \
  "s_nop 1\n\t"
#define CONV_CLOB "v24","v25","v26","v27","v28","v29","v30","v31","v32","v33","v34","v35","v36","v37","v38","v39","v40","v41","v42","v43","v44","v45","v46","v47","v48","v49","v50","v51","v52","v53","v54","v55","v56","v57","v58","v59","v60","v61","v62","v63","v64","v65","v66","v67","v68","v69","v70","v71","v72","v73","v74","v75","v76","v77","v78","v79","v80","v81","v82","v83","v84","v85","v86","v87","v88","v89","v90","v91","v92","v93","v94","v95","v96","v97","v98","v99","v100","v101","v102","v103","v104","v105","v106","v107","v108","v109","v110","v111","v112","v113","v114","v115","v116","v117","v118","v119","v120","v121","v122","v123","v124","v125","v126","v127","v128","v129","v130","v131","v132","v133","v134","v135","v136","v137","v138","v139","v140","v141","v142","v143","v144","v145","v146","v147","v148","v149","v150","v151","v152","v153","v154","v155","v156","v157","v158","v159","v176","v177","v178","v179","v180","v181","v182","v183","v184","v185","v186","v187","v188","v189","v190","v191","v192","v193","v194","v195","v196","v197","v198","v199","v200","v201","v202","v203","v204","v205","v206","v207","v208","v209","v210","v211","v212","v213","v214","v215"
#define OPROJ_ASM \
  "buffer_load_dwordx4 v[96:99], %[oa], %[rs], %[so] offen offset:0 sc1\n\t" \
  "buffer_load_dwordx4 v[100:103], %[oa], %[rs], %[so] offen offset:1024 sc1\n\t" \
  "buffer_load_dwordx4 v[104:107], %[oa], %[rs], %[so] offen offset:2048 sc1\n\t" \
  "buffer_load_dwordx4 v[108:111], %[oa], %[rs], %[so] offen offset:3072 sc1\n\t" \
  "s_add_u32 %[so], %[so], 0x1000\n\t" \
  "s_and_b32 %[so], %[so], 0x7fff\n\t" \
  "buffer_load_dwordx4 v[112:115], %[oa], %[rs], %[so] offen offset:0 sc1\n\t" \
  "buffer_load_dwordx4 v[116:119], %[oa], %[rs], %[so] offen offset:1024 sc1\n\t" \
  "buffer_load_dwordx4 v[120:123], %[oa], %[rs], %[so] offen offset:2048 sc1\n\t" \
  "buffer_load_dwordx4 v[124:127], %[oa], %[rs], %[so] offen offset:3072 sc1\n\t" \
  "s_add_u32 %[so], %[so], 0x1000\n\t" \
  "s_and_b32 %[so], %[so], 0x7fff\n\t" \
  "ds_read_b64_tr_b16 v[32:33], %[w0] offset:0\n\t" \
  "ds_read_b64_tr_b16 v[34:35], %[w0] offset:1024\n\t" \
  "ds_read_b64_tr_b16 v[36:37], %[w1] offset:0\n\t" \
  "ds_read_b64_tr_b16 v[38:39], %[w1] offset:1024\n\t" \
  "ds_read_b64_tr_b16 v[40:41], %[w2] offset:0\n\t" \
  "ds_read_b64_tr_b16 v[42:43], %[w2] offset:1024\n\t" \
  "ds_read_b64_tr_b16 v[44:45], %[w3] offset:0\n\t" \
  "ds_read_b64_tr_b16 v[46:47], %[w3] offset:1024\n\t" \
  "ds_read_b64_tr_b16 v[48:49], %[w0] offset:4096\n\t" \
  "ds_read_b64_tr_b16 v[50:51], %[w0] offset:5120\n\t" \
  "ds_read_b64_tr_b16 v[52:53], %[w1] offset:4096\n\t" \
  "ds_read_b64_tr_b16 v[54:55], %[w1] offset:5120\n\t" \
  "ds_read_b64_tr_b16 v[56:57], %[w2] offset:4096\n\t" \
  "ds_read_b64_tr_b16 v[58:59], %[w2] offset:5120\n\t" \
  "s_waitcnt vmcnt(7) lgkmcnt(12)\n\t" \
  "v_mfma_f32_16x16x32_bf16 v[64:67], v[32:35], v[96:99], 0\n\t" \
  "ds_read_b64_tr_b16 v[60:61], %[w3] offset:4096\n\t" \
  "s_waitcnt vmcnt(6)\n\t" \
  "v_mfma_f32_16x16x32_bf16 v[80:83], v[32:35], v[100:103], 0\n\t" \
  "buffer_load_dwordx4 v[128:131], %[oa], %[rs], %[so] offen offset:0 sc1\n\t" \
  "s_waitcnt lgkmcnt(11)\n\t" \
  "v_mfma_f32_16x16x32_bf16 v[68:71], v[36:39], v[96:99], 0\n\t" \
  "ds_read_b64_tr_b16 v[62:63], %[w3] offset:5120\n\t" \
  "v_mfma_f32_16x16x32_bf16 v[84:87], v[36:39], v[100:103], 0\n\t" \
  "ds_read_b64_tr_b16 v[32:33], %[w0] offset:8192\n\t" \
  "s_waitcnt lgkmcnt(11)\n\t" \
  "v_mfma_f32_16x16x32_bf16 v[72:75], v[40:43], v[96:99], 0\n\t" \
  "s_cmp_eq_u32 %[dob], 0\n\t" \
  "s_cbranch_scc1 7f\n\t" \
  "s_barrier\n\t" \
  "7:\n\t" \
  "v_mfma_f32_16x16x32_bf16 v[88:91], v[40:43], v[100:103], 0\n\t" \
  "buffer_load_dwordx4 v[132:135], %[oa], %[rs], %[so] offen offset:1024 sc1\n\t" \
  "s_waitcnt lgkmcnt(9)\n\t" \
  "v_mfma_f32_16x16x32_bf16 v[76:79], v[44:47], v[96:99], 0\n\t" \
  "ds_read_b64_tr_b16 v[34:35], %[w0] offset:9216\n\t" \
  "v_mfma_f32_16x16x32_bf16 v[92:95], v[44:47], v[100:103], 0\n\t" \
  "ds_read_b64_tr_b16 v[36:37], %[w1] offset:8192\n\t" \
  "s_waitcnt vmcnt(7) lgkmcnt(9)\n\t" \
  "v_mfma_f32_16x16x32_bf16 v[64:67], v[48:51], v[104:107], v[64:67]\n\t" \
  "ds_read_b64_tr_b16 v[38:39], %[w1] offset:9216\n\t" \
  "s_waitcnt vmcnt(6)\n\t" \
  "v_mfma_f32_16x16x32_bf16 v[80:83], v[48:51], v[108:111], v[80:83]\n\t" \
  "buffer_load_dwordx4 v[136:139], %[oa], %[rs], %[so] offen offset:2048 sc1\n\t" \
  "s_waitcnt lgkmcnt(8)\n\t" \
  "v_mfma_f32_16x16x32_bf16 v[68:71], v[52:55], v[104:107], v[68:71]\n\t" \
  "ds_read_b64_tr_b16 v[40:41], %[w2] offset:8192\n\t" \
  "v_mfma_f32_16x16x32_bf16 v[84:87], v[52:55], v[108:111], v[84:87]\n\t" \
  "ds_read_b64_tr_b16 v[42:43], %[w2] offset:9216\n\t" \
  "s_waitcnt lgkmcnt(8)\n\t" \
  "v_mfma_f32_16x16x32_bf16 v[72:75], v[56:59], v[104:107], v[72:75]\n\t" \
  "ds_read_b64_tr_b16 v[44:45], %[w3] offset:8192\n\t" \
  "v_mfma_f32_16x16x32_bf16 v[88:91], v[56:59], v[108:111], v[88:91]\n\t" \
  "buffer_load_dwordx4 v[140:143], %[oa], %[rs], %[so] offen offset:3072 sc1\n\t" \
  "s_add_u32 %[so], %[so], 0x1000\n\t" \
  "s_and_b32 %[so], %[so], 0x7fff\n\t" \
  "s_waitcnt lgkmcnt(7)\n\t" \
  "v_mfma_f32_16x16x32_bf16 v[76:79], v[60:63], v[104:107], v[76:79]\n\t" \
  "ds_read_b64_tr_b16 v[46:47], %[w3] offset:9216\n\t" \
  "v_mfma_f32_16x16x32_bf16 v[92:95], v[60:63], v[108:111], v[92:95]\n\t" \
  "ds_read_b64_tr_b16 v[48:49], %[w0] offset:12288\n\t" \
  "s_waitcnt vmcnt(7) lgkmcnt(7)\n\t" \
  "v_mfma_f32_16x16x32_bf16 v[64:67], v[32:35], v[112:115], v[64:67]\n\t" \
  "ds_read_b64_tr_b16 v[50:51], %[w0] offset:13312\n\t" \
  "s_waitcnt vmcnt(6)\n\t" \
  "v_mfma_f32_16x16x32_bf16 v[80:83], v[32:35], v[116:119], v[80:83]\n\t" \
  "ds_read_b64_tr_b16 v[52:53], %[w1] offset:12288\n\t" \
  "buffer_load_dwordx4 v[144:147], %[oa], %[rs], %[so] offen offset:0 sc1\n\t" \
  "s_waitcnt lgkmcnt(7)\n\t" \
  "v_mfma_f32_16x16x32_bf16 v[68:71], v[36:39], v[112:115], v[68:71]\n\t" \
  "ds_read_b64_tr_b16 v[54:55], %[w1] offset:13312\n\t" \
  "v_mfma_f32_16x16x32_bf16 v[84:87], v[36:39], v[116:119], v[84:87]\n\t" \
  "ds_read_b64_tr_b16 v[56:57], %[w2] offset:12288\n\t" \
  "s_waitcnt lgkmcnt(7)\n\t" \
  "v_mfma_f32_16x16x32_bf16 v[72:75], v[40:43], v[112:115], v[72:75]\n\t" \
  "ds_read_b64_tr_b16 v[58:59], %[w2] offset:13312\n\t" \
  "v_mfma_f32_16x16x32_bf16 v[88:91], v[40:43], v[116:119], v[88:91]\n\t" \
  "ds_read_b64_tr_b16 v[60:61], %[w3] offset:12288\n\t" \
  "buffer_load_dwordx4 v[148:151], %[oa], %[rs], %[so] offen offset:1024 sc1\n\t" \
  "s_waitcnt lgkmcnt(7)\n\t" \
  "v_mfma_f32_16x16x32_bf16 v[76:79], v[44:47], v[112:115], v[76:79]\n\t" \
  "ds_read_b64_tr_b16 v[62:63], %[w3] offset:13312\n\t" \
  "v_mfma_f32_16x16x32_bf16 v[92:95], v[44:47], v[116:119], v[92:95]\n\t" \
  "ds_read_b64_tr_b16 v[32:33], %[w0] offset:16384\n\t" \
  "s_waitcnt vmcnt(7) lgkmcnt(7)\n\t" \
  "v_mfma_f32_16x16x32_bf16 v[64:67], v[48:51], v[120:123], v[64:67]\n\t" \
  "ds_read_b64_tr_b16 v[34:35], %[w0] offset:17408\n\t" \
  "s_waitcnt vmcnt(6)\n\t" \
  "v_mfma_f32_16x16x32_bf16 v[80:83], v[48:51], v[124:127], v[80:83]\n\t" \
  "ds_read_b64_tr_b16 v[36:37], %[w1] offset:16384\n\t" \
  "buffer_load_dwordx4 v[152:155], %[oa], %[rs], %[so] offen offset:2048 sc1\n\t" \
  "s_waitcnt lgkmcnt(7)\n\t" \
  "v_mfma_f32_16x16x32_bf16 v[68:71], v[52:55], v[120:123], v[68:71]\n\t" \
  "ds_read_b64_tr_b16 v[38:39], %[w1] offset:17408\n\t" \
  "v_mfma_f32_16x16x32_bf16 v[84:87], v[52:55], v[124:127], v[84:87]\n\t" \
  "ds_read_b64_tr_b16 v[40:41], %[w2] offset:16384\n\t" \
  "s_waitcnt lgkmcnt(7)\n\t" \
  "v_mfma_f32_16x16x32_bf16 v[72:75], v[56:59], v[120:123], v[72:75]\n\t" \
  "ds_read_b64_tr_b16 v[42:43], %[w2] offset:17408\n\t" \
  "v_mfma_f32_16x16x32_bf16 v[88:91], v[56:59], v[124:127], v[88:91]\n\t" \
  "buffer_load_dwordx4 v[156:159], %[oa], %[rs], %[so] offen offset:3072 sc1\n\t" \
  "s_add_u32 %[so], %[so], 0x1000\n\t" \
  "s_and_b32 %[so], %[so], 0x7fff\n\t" \
  "s_waitcnt lgkmcnt(6)\n\t" \
  "v_mfma_f32_16x16x32_bf16 v[76:79], v[60:63], v[120:123], v[76:79]\n\t" \
  "ds_read_b64_tr_b16 v[44:45], %[w3] offset:16384\n\t" \
  "v_mfma_f32_16x16x32_bf16 v[92:95], v[60:63], v[124:127], v[92:95]\n\t" \
  "ds_read_b64_tr_b16 v[46:47], %[w3] offset:17408\n\t" \
  "s_waitcnt vmcnt(7) lgkmcnt(6)\n\t" \
  "v_mfma_f32_16x16x32_bf16 v[64:67], v[32:35], v[128:131], v[64:67]\n\t" \
  "ds_read_b64_tr_b16 v[48:49], %[w0] offset:20480\n\t" \
  "s_waitcnt vmcnt(6)\n\t" \
  "v_mfma_f32_16x16x32_bf16 v[80:83], v[32:35], v[132:135], v[80:83]\n\t" \
  "ds_read_b64_tr_b16 v[50:51], %[w0] offset:21504\n\t" \
  "buffer_load_dwordx4 v[160:163], %[oa], %[rs], %[so] offen offset:0 sc1\n\t" \
  "s_waitcnt lgkmcnt(6)\n\t" \
  "v_mfma_f32_16x16x32_bf16 v[68:71], v[36:39], v[128:131], v[68:71]\n\t" \
  "ds_read_b64_tr_b16 v[52:53], %[w1] offset:20480\n\t" \
  "v_mfma_f32_16x16x32_bf16 v[84:87], v[36:39], v[132:135], v[84:87]\n\t" \
  "ds_read_b64_tr_b16 v[54:55], %[w1] offset:21504\n\t" \
  "s_waitcnt lgkmcnt(6)\n\t" \
  "v_mfma_f32_16x16x32_bf16 v[72:75], v[40:43], v[128:131], v[72:75]\n\t" \
  "ds_read_b64_tr_b16 v[56:57], %[w2] offset:20480\n\t" \
  "v_mfma_f32_16x16x32_bf16 v[88:91], v[40:43], v[132:135], v[88:91]\n\t" \
  "ds_read_b64_tr_b16 v[58:59], %[w2] offset:21504\n\t" \
  "buffer_load_dwordx4 v[164:167], %[oa], %[rs], %[so] offen offset:1024 sc1\n\t" \
  "s_waitcnt lgkmcnt(6)\n\t" \
  "v_mfma_f32_16x16x32_bf16 v[76:79], v[44:47], v[128:131], v[76:79]\n\t" \
  "ds_read_b64_tr_b16 v[60:61], %[w3] offset:20480\n\t" \
  "v_mfma_f32_16x16x32_bf16 v[92:95], v[44:47], v[132:135], v[92:95]\n\t" \
  "ds_read_b64_tr_b16 v[62:63], %[w3] offset:21504\n\t" \
  "s_waitcnt vmcnt(7) lgkmcnt(6)\n\t" \
  "v_mfma_f32_16x16x32_bf16 v[64:67], v[48:51], v[136:139], v[64:67]\n\t" \
  "ds_read_b64_tr_b16 v[32:33], %[w0] offset:24576\n\t" \
  "s_waitcnt vmcnt(6)\n\t" \
  "v_mfma_f32_16x16x32_bf16 v[80:83], v[48:51], v[140:143], v[80:83]\n\t" \
  "ds_read_b64_tr_b16 v[34:35], %[w0] offset:25600\n\t" \
  "buffer_load_dwordx4 v[168:171], %[oa], %[rs], %[so] offen offset:2048 sc1\n\t" \
  "s_waitcnt lgkmcnt(6)\n\t" \
  "v_mfma_f32_16x16x32_bf16 v[68:71], v[52:55], v[136:139], v[68:71]\n\t" \
  "ds_read_b64_tr_b16 v[36:37], %[w1] offset:24576\n\t" \
  "v_mfma_f32_16x16x32_bf16 v[84:87], v[52:55], v[140:143], v[84:87]\n\t" \
  "ds_read_b64_tr_b16 v[38:39], %[w1] offset:25600\n\t" \
  "s_waitcnt lgkmcnt(6)\n\t" \
  "v_mfma_f32_16x16x32_bf16 v[72:75], v[56:59], v[136:139], v[72:75]\n\t" \
  "ds_read_b64_tr_b16 v[40:41], %[w2] offset:24576\n\t" \
  "v_mfma_f32_16x16x32_bf16 v[88:91], v[56:59], v[140:143], v[88:91]\n\t" \
  "buffer_load_dwordx4 v[172:175], %[oa], %[rs], %[so] offen offset:3072 sc1\n\t" \
  "s_add_u32 %[so], %[so], 0x1000\n\t" \
  "s_and_b32 %[so], %[so], 0x7fff\n\t" \
  "s_waitcnt lgkmcnt(5)\n\t" \
  "v_mfma_f32_16x16x32_bf16 v[76:79], v[60:63], v[136:139], v[76:79]\n\t" \
  "ds_read_b64_tr_b16 v[42:43], %[w2] offset:25600\n\t" \
  "v_mfma_f32_16x16x32_bf16 v[92:95], v[60:63], v[140:143], v[92:95]\n\t" \
  "ds_read_b64_tr_b16 v[44:45], %[w3] offset:24576\n\t" \
  "s_waitcnt vmcnt(7) lgkmcnt(5)\n\t" \
  "v_mfma_f32_16x16x32_bf16 v[64:67], v[32:35], v[144:147], v[64:67]\n\t" \
  "ds_read_b64_tr_b16 v[46:47], %[w3] offset:25600\n\t" \
  "s_waitcnt vmcnt(6)\n\t" \
  "v_mfma_f32_16x16x32_bf16 v[80:83], v[32:35], v[148:151], v[80:83]\n\t" \
  "ds_read_b64_tr_b16 v[48:49], %[w0] offset:28672\n\t" \
  "buffer_load_dwordx4 v[176:179], %[oa], %[rs], %[so] offen offset:0 sc1\n\t" \
  "s_waitcnt lgkmcnt(5)\n\t" \
  "v_mfma_f32_16x16x32_bf16 v[68:71], v[36:39], v[144:147], v[68:71]\n\t" \
  "ds_read_b64_tr_b16 v[50:51], %[w0] offset:29696\n\t" \
  "v_mfma_f32_16x16x32_bf16 v[84:87], v[36:39], v[148:151], v[84:87]\n\t" \
  "ds_read_b64_tr_b16 v[52:53], %[w1] offset:28672\n\t" \
  "s_waitcnt lgkmcnt(5)\n\t" \
  "v_mfma_f32_16x16x32_bf16 v[72:75], v[40:43], v[144:147], v[72:75]\n\t" \
  "ds_read_b64_tr_b16 v[54:55], %[w1] offset:29696\n\t" \
  "v_mfma_f32_16x16x32_bf16 v[88:91], v[40:43], v[148:151], v[88:91]\n\t" \
  "ds_read_b64_tr_b16 v[56:57], %[w2] offset:28672\n\t" \
  "buffer_load_dwordx4 v[180:183], %[oa], %[rs], %[so] offen offset:1024 sc1\n\t" \
  "s_waitcnt lgkmcnt(5)\n\t" \
  "v_mfma_f32_16x16x32_bf16 v[76:79], v[44:47], v[144:147], v[76:79]\n\t" \
  "ds_read_b64_tr_b16 v[58:59], %[w2] offset:29696\n\t" \
  "v_mfma_f32_16x16x32_bf16 v[92:95], v[44:47], v[148:151], v[92:95]\n\t" \
  "ds_read_b64_tr_b16 v[60:61], %[w3] offset:28672\n\t" \
  "s_waitcnt vmcnt(7) lgkmcnt(5)\n\t" \
  "v_mfma_f32_16x16x32_bf16 v[64:67], v[48:51], v[152:155], v[64:67]\n\t" \
  "ds_read_b64_tr_b16 v[62:63], %[w3] offset:29696\n\t" \
  "s_waitcnt vmcnt(6)\n\t" \
  "v_mfma_f32_16x16x32_bf16 v[80:83], v[48:51], v[156:159], v[80:83]\n\t" \
  "ds_read_b64_tr_b16 v[32:33], %[w0] offset:32768\n\t" \
  "buffer_load_dwordx4 v[184:187], %[oa], %[rs], %[so] offen offset:2048 sc1\n\t" \
  "s_waitcnt lgkmcnt(5)\n\t" \
  "v_mfma_f32_16x16x32_bf16 v[68:71], v[52:55], v[152:155], v[68:71]\n\t" \
  "ds_read_b64_tr_b16 v[34:35], %[w0] offset:33792\n\t" \
  "v_mfma_f32_16x16x32_bf16 v[84:87], v[52:55], v[156:159], v[84:87]\n\t" \
  "ds_read_b64_tr_b16 v[36:37], %[w1] offset:32768\n\t" \
  "s_waitcnt lgkmcnt(5)\n\t" \
  "v_mfma_f32_16x16x32_bf16 v[72:75], v[56:59], v[152:155], v[72:75]\n\t" \
  "ds_read_b64_tr_b16 v[38:39], %[w1] offset:33792\n\t" \
  "v_mfma_f32_16x16x32_bf16 v[88:91], v[56:59], v[156:159], v[88:91]\n\t" \
  "buffer_load_dwordx4 v[188:191], %[oa], %[rs], %[so] offen offset:3072 sc1\n\t" \
  "s_add_u32 %[so], %[so], 0x1000\n\t" \
  "s_and_b32 %[so], %[so], 0x7fff\n\t" \
  "s_waitcnt lgkmcnt(4)\n\t" \
  "v_mfma_f32_16x16x32_bf16 v[76:79], v[60:63], v[152:155], v[76:79]\n\t" \
  "ds_read_b64_tr_b16 v[40:41], %[w2] offset:32768\n\t" \
  "v_mfma_f32_16x16x32_bf16 v[92:95], v[60:63], v[156:159], v[92:95]\n\t" \
  "ds_read_b64_tr_b16 v[42:43], %[w2] offset:33792\n\t" \
  "s_waitcnt vmcnt(7) lgkmcnt(4)\n\t" \
  "v_mfma_f32_16x16x32_bf16 v[64:67], v[32:35], v[160:163], v[64:67]\n\t" \
  "ds_read_b64_tr_b16 v[44:45], %[w3] offset:32768\n\t" \
  "s_waitcnt vmcnt(6)\n\t" \
  "v_mfma_f32_16x16x32_bf16 v[80:83], v[32:35], v[164:167], v[80:83]\n\t" \
  "ds_read_b64_tr_b16 v[46:47], %[w3] offset:33792\n\t" \
  "buffer_load_dwordx4 v[192:195], %[oa], %[rs], %[so] offen offset:0 sc1\n\t" \
  "s_waitcnt lgkmcnt(4)\n\t" \
  "v_mfma_f32_16x16x32_bf16 v[68:71], v[36:39], v[160:163], v[68:71]\n\t" \
  "ds_read_b64_tr_b16 v[48:49], %[w0] offset:36864\n\t" \
  "v_mfma_f32_16x16x32_bf16 v[84:87], v[36:39], v[164:167], v[84:87]\n\t" \
  "ds_read_b64_tr_b16 v[50:51], %[w0] offset:37888\n\t" \
  "s_waitcnt lgkmcnt(4)\n\t" \
  "v_mfma_f32_16x16x32_bf16 v[72:75], v[40:43], v[160:163], v[72:75]\n\t" \
  "ds_read_b64_tr_b16 v[52:53], %[w1] offset:36864\n\t" \
  "v_mfma_f32_16x16x32_bf16 v[88:91], v[40:43], v[164:167], v[88:91]\n\t" \
  "ds_read_b64_tr_b16 v[54:55], %[w1] offset:37888\n\t" \
  "buffer_load_dwordx4 v[196:199], %[oa], %[rs], %[so] offen offset:1024 sc1\n\t" \
  "s_waitcnt lgkmcnt(4)\n\t" \
  "v_mfma_f32_16x16x32_bf16 v[76:79], v[44:47], v[160:163], v[76:79]\n\t" \
  "ds_read_b64_tr_b16 v[56:57], %[w2] offset:36864\n\t" \
  "v_mfma_f32_16x16x32_bf16 v[92:95], v[44:47], v[164:167], v[92:95]\n\t" \
  "ds_read_b64_tr_b16 v[58:59], %[w2] offset:37888\n\t" \
  "s_waitcnt vmcnt(7) lgkmcnt(4)\n\t" \
  "v_mfma_f32_16x16x32_bf16 v[64:67], v[48:51], v[168:171], v[64:67]\n\t" \
  "ds_read_b64_tr_b16 v[60:61], %[w3] offset:36864\n\t" \
  "s_waitcnt vmcnt(6)\n\t" \
  "v_mfma_f32_16x16x32_bf16 v[80:83], v[48:51], v[172:175], v[80:83]\n\t" \
  "ds_read_b64_tr_b16 v[62:63], %[w3] offset:37888\n\t" \
  "buffer_load_dwordx4 v[200:203], %[oa], %[rs], %[so] offen offset:2048 sc1\n\t" \
  "s_waitcnt lgkmcnt(4)\n\t" \
  "v_mfma_f32_16x16x32_bf16 v[68:71], v[52:55], v[168:171], v[68:71]\n\t" \
  "ds_read_b64_tr_b16 v[32:33], %[w0] offset:40960\n\t" \
  "v_mfma_f32_16x16x32_bf16 v[84:87], v[52:55], v[172:175], v[84:87]\n\t" \
  "ds_read_b64_tr_b16 v[34:35], %[w0] offset:41984\n\t" \
  "s_waitcnt lgkmcnt(4)\n\t" \
  "v_mfma_f32_16x16x32_bf16 v[72:75], v[56:59], v[168:171], v[72:75]\n\t" \
  "ds_read_b64_tr_b16 v[36:37], %[w1] offset:40960\n\t" \
  "v_mfma_f32_16x16x32_bf16 v[88:91], v[56:59], v[172:175], v[88:91]\n\t" \
  "buffer_load_dwordx4 v[204:207], %[oa], %[rs], %[so] offen offset:3072 sc1\n\t" \
  "s_add_u32 %[so], %[so], 0x1000\n\t" \
  "s_and_b32 %[so], %[so], 0x7fff\n\t" \
  "s_waitcnt lgkmcnt(3)\n\t" \
  "v_mfma_f32_16x16x32_bf16 v[76:79], v[60:63], v[168:171], v[76:79]\n\t" \
  "ds_read_b64_tr_b16 v[38:39], %[w1] offset:41984\n\t" \
  "v_mfma_f32_16x16x32_bf16 v[92:95], v[60:63], v[172:175], v[92:95]\n\t" \
  "ds_read_b64_tr_b16 v[40:41], %[w2] offset:40960\n\t" \
  "s_waitcnt vmcnt(7) lgkmcnt(3)\n\t" \
  "v_mfma_f32_16x16x32_bf16 v[64:67], v[32:35], v[176:179], v[64:67]\n\t" \
  "ds_read_b64_tr_b16 v[42:43], %[w2] offset:41984\n\t" \
  "s_waitcnt vmcnt(6)\n\t" \
  "v_mfma_f32_16x16x32_bf16 v[80:83], v[32:35], v[180:183], v[80:83]\n\t" \
  "ds_read_b64_tr_b16 v[44:45], %[w3] offset:40960\n\t" \
  "buffer_load_dwordx4 v[208:211], %[oa], %[rs], %[so] offen offset:0 sc1\n\t" \
  "s_waitcnt lgkmcnt(3)\n\t" \
  "v_mfma_f32_16x16x32_bf16 v[68:71], v[36:39], v[176:179], v[68:71]\n\t" \
  "ds_read_b64_tr_b16 v[46:47], %[w3] offset:41984\n\t" \
  "v_mfma_f32_16x16x32_bf16 v[84:87], v[36:39], v[180:183], v[84:87]\n\t" \
  "ds_read_b64_tr_b16 v[48:49], %[w0] offset:45056\n\t" \
  "s_waitcnt lgkmcnt(3)\n\t" \
  "v_mfma_f32_16x16x32_bf16 v[72:75], v[40:43], v[176:179], v[72:75]\n\t" \
  "ds_read_b64_tr_b16 v[50:51], %[w0] offset:46080\n\t" \
  "v_mfma_f32_16x16x32_bf16 v[88:91], v[40:43], v[180:183], v[88:91]\n\t" \
  "ds_read_b64_tr_b16 v[52:53], %[w1] offset:45056\n\t" \
  "buffer_load_dwordx4 v[212:215], %[oa], %[rs], %[so] offen offset:1024 sc1\n\t" \
  "s_waitcnt lgkmcnt(3)\n\t" \
  "v_mfma_f32_16x16x32_bf16 v[76:79], v[44:47], v[176:179], v[76:79]\n\t" \
  "ds_read_b64_tr_b16 v[54:55], %[w1] offset:46080\n\t" \
  "v_mfma_f32_16x16x32_bf16 v[92:95], v[44:47], v[180:183], v[92:95]\n\t" \
  "ds_read_b64_tr_b16 v[56:57], %[w2] offset:45056\n\t" \
  "s_waitcnt vmcnt(7) lgkmcnt(3)\n\t" \
  "v_mfma_f32_16x16x32_bf16 v[64:67], v[48:51], v[184:187], v[64:67]\n\t" \
  "ds_read_b64_tr_b16 v[58:59], %[w2] offset:46080\n\t" \
  "s_waitcnt vmcnt(6)\n\t" \
  "v_mfma_f32_16x16x32_bf16 v[80:83], v[48:51], v[188:191], v[80:83]\n\t" \
  "ds_read_b64_tr_b16 v[60:61], %[w3] offset:45056\n\t" \
  "buffer_load_dwordx4 v[216:219], %[oa], %[rs], %[so] offen offset:2048 sc1\n\t" \
  "s_waitcnt lgkmcnt(3)\n\t" \
  "v_mfma_f32_16x16x32_bf16 v[68:71], v[52:55], v[184:187], v[68:71]\n\t" \
  "ds_read_b64_tr_b16 v[62:63], %[w3] offset:46080\n\t" \
  "v_mfma_f32_16x16x32_bf16 v[84:87], v[52:55], v[188:191], v[84:87]\n\t" \
  "ds_read_b64_tr_b16 v[32:33], %[w0] offset:49152\n\t" \
  "s_waitcnt lgkmcnt(3)\n\t" \
  "v_mfma_f32_16x16x32_bf16 v[72:75], v[56:59], v[184:187], v[72:75]\n\t" \
  "ds_read_b64_tr_b16 v[34:35], %[w0] offset:50176\n\t" \
  "v_mfma_f32_16x16x32_bf16 v[88:91], v[56:59], v[188:191], v[88:91]\n\t" \
  "ds_read_b64_tr_b16 v[36:37], %[w1] offset:49152\n\t" \
  "buffer_load_dwordx4 v[220:223], %[oa], %[rs], %[so] offen offset:3072 sc1\n\t" \
  "s_waitcnt lgkmcnt(3)\n\t" \
  "v_mfma_f32_16x16x32_bf16 v[76:79], v[60:63], v[184:187], v[76:79]\n\t" \
  "ds_read_b64_tr_b16 v[38:39], %[w1] offset:50176\n\t" \
  "v_mfma_f32_16x16x32_bf16 v[92:95], v[60:63], v[188:191], v[92:95]\n\t" \
  "ds_read_b64_tr_b16 v[40:41], %[w2] offset:49152\n\t" \
  "s_waitcnt vmcnt(7) lgkmcnt(3)\n\t" \
  "v_mfma_f32_16x16x32_bf16 v[64:67], v[32:35], v[192:195], v[64:67]\n\t" \
  "ds_read_b64_tr_b16 v[42:43], %[w2] offset:50176\n\t" \
  "s_waitcnt vmcnt(6)\n\t" \
  "v_mfma_f32_16x16x32_bf16 v[80:83], v[32:35], v[196:199], v[80:83]\n\t" \
  "ds_read_b64_tr_b16 v[44:45], %[w3] offset:49152\n\t" \
  "s_waitcnt lgkmcnt(3)\n\t" \
  "v_mfma_f32_16x16x32_bf16 v[68:71], v[36:39], v[192:195], v[68:71]\n\t" \
  "ds_read_b64_tr_b16 v[46:47], %[w3] offset:50176\n\t" \
  "v_mfma_f32_16x16x32_bf16 v[84:87], v[36:39], v[196:199], v[84:87]\n\t" \
  "ds_read_b64_tr_b16 v[48:49], %[w0] offset:53248\n\t" \
  "s_waitcnt lgkmcnt(3)\n\t" \
  "v_mfma_f32_16x16x32_bf16 v[72:75], v[40:43], v[192:195], v[72:75]\n\t" \
  "ds_read_b64_tr_b16 v[50:51], %[w0] offset:54272\n\t" \
  "v_mfma_f32_16x16x32_bf16 v[88:91], v[40:43], v[196:199], v[88:91]\n\t" \
  "ds_read_b64_tr_b16 v[52:53], %[w1] offset:53248\n\t" \
  "s_waitcnt lgkmcnt(3)\n\t" \
  "v_mfma_f32_16x16x32_bf16 v[76:79], v[44:47], v[192:195], v[76:79]\n\t" \
  "ds_read_b64_tr_b16 v[54:55], %[w1] offset:54272\n\t" \
  "v_mfma_f32_16x16x32_bf16 v[92:95], v[44:47], v[196:199], v[92:95]\n\t" \
  "ds_read_b64_tr_b16 v[56:57], %[w2] offset:53248\n\t" \
  "s_waitcnt vmcnt(5) lgkmcnt(3)\n\t" \
  "v_mfma_f32_16x16x32_bf16 v[64:67], v[48:51], v[200:203], v[64:67]\n\t" \
  "ds_read_b64_tr_b16 v[58:59], %[w2] offset:54272\n\t" \
  "s_waitcnt vmcnt(4)\n\t" \
  "v_mfma_f32_16x16x32_bf16 v[80:83], v[48:51], v[204:207], v[80:83]\n\t" \
  "ds_read_b64_tr_b16 v[60:61], %[w3] offset:53248\n\t" \
  "s_waitcnt lgkmcnt(3)\n\t" \
  "v_mfma_f32_16x16x32_bf16 v[68:71], v[52:55], v[200:203], v[68:71]\n\t" \
  "ds_read_b64_tr_b16 v[62:63], %[w3] offset:54272\n\t" \
  "v_mfma_f32_16x16x32_bf16 v[84:87], v[52:55], v[204:207], v[84:87]\n\t" \
  "ds_read_b64_tr_b16 v[32:33], %[w0] offset:57344\n\t" \
  "s_waitcnt lgkmcnt(3)\n\t" \
  "v_mfma_f32_16x16x32_bf16 v[72:75], v[56:59], v[200:203], v[72:75]\n\t" \
  "ds_read_b64_tr_b16 v[34:35], %[w0] offset:58368\n\t" \
  "v_mfma_f32_16x16x32_bf16 v[88:91], v[56:59], v[204:207], v[88:91]\n\t" \
  "ds_read_b64_tr_b16 v[36:37], %[w1] offset:57344\n\t" \
  "s_waitcnt lgkmcnt(3)\n\t" \
  "v_mfma_f32_16x16x32_bf16 v[76:79], v[60:63], v[200:203], v[76:79]\n\t" \
  "ds_read_b64_tr_b16 v[38:39], %[w1] offset:58368\n\t" \
  "v_mfma_f32_16x16x32_bf16 v[92:95], v[60:63], v[204:207], v[92:95]\n\t" \
  "ds_read_b64_tr_b16 v[40:41], %[w2] offset:57344\n\t" \
  "s_waitcnt vmcnt(3) lgkmcnt(3)\n\t" \
  "v_mfma_f32_16x16x32_bf16 v[64:67], v[32:35], v[208:211], v[64:67]\n\t" \
  "ds_read_b64_tr_b16 v[42:43], %[w2] offset:58368\n\t" \
  "s_waitcnt vmcnt(2)\n\t" \
  "v_mfma_f32_16x16x32_bf16 v[80:83], v[32:35], v[212:215], v[80:83]\n\t" \
  "ds_read_b64_tr_b16 v[44:45], %[w3] offset:57344\n\t" \
  "s_waitcnt lgkmcnt(3)\n\t" \
  "v_mfma_f32_16x16x32_bf16 v[68:71], v[36:39], v[208:211], v[68:71]\n\t" \
  "ds_read_b64_tr_b16 v[46:47], %[w3] offset:58368\n\t" \
  "v_mfma_f32_16x16x32_bf16 v[84:87], v[36:39], v[212:215], v[84:87]\n\t" \
  "ds_read_b64_tr_b16 v[48:49], %[w0] offset:61440\n\t" \
  "s_waitcnt lgkmcnt(3)\n\t" \
  "v_mfma_f32_16x16x32_bf16 v[72:75], v[40:43], v[208:211], v[72:75]\n\t" \
  "ds_read_b64_tr_b16 v[50:51], %[w0] offset:62464\n\t" \
  "v_mfma_f32_16x16x32_bf16 v[88:91], v[40:43], v[212:215], v[88:91]\n\t" \
  "ds_read_b64_tr_b16 v[52:53], %[w1] offset:61440\n\t" \
  "s_waitcnt lgkmcnt(3)\n\t" \
  "v_mfma_f32_16x16x32_bf16 v[76:79], v[44:47], v[208:211], v[76:79]\n\t" \
  "ds_read_b64_tr_b16 v[54:55], %[w1] offset:62464\n\t" \
  "v_mfma_f32_16x16x32_bf16 v[92:95], v[44:47], v[212:215], v[92:95]\n\t" \
  "ds_read_b64_tr_b16 v[56:57], %[w2] offset:61440\n\t" \
  "s_waitcnt vmcnt(1) lgkmcnt(3)\n\t" \
  "v_mfma_f32_16x16x32_bf16 v[64:67], v[48:51], v[216:219], v[64:67]\n\t" \
  "ds_read_b64_tr_b16 v[58:59], %[w2] offset:62464\n\t" \
  "s_waitcnt vmcnt(0)\n\t" \
  "v_mfma_f32_16x16x32_bf16 v[80:83], v[48:51], v[220:223], v[80:83]\n\t" \
  "ds_read_b64_tr_b16 v[60:61], %[w3] offset:61440\n\t" \
  "s_waitcnt lgkmcnt(3)\n\t" \
  "v_mfma_f32_16x16x32_bf16 v[68:71], v[52:55], v[216:219], v[68:71]\n\t" \
  "ds_read_b64_tr_b16 v[62:63], %[w3] offset:62464\n\t" \
  "v_mfma_f32_16x16x32_bf16 v[84:87], v[52:55], v[220:223], v[84:87]\n\t" \
  "s_waitcnt lgkmcnt(2)\n\t" \
  "v_mfma_f32_16x16x32_bf16 v[72:75], v[56:59], v[216:219], v[72:75]\n\t" \
  "v_mfma_f32_16x16x32_bf16 v[88:91], v[56:59], v[220:223], v[88:91]\n\t" \
  "s_waitcnt lgkmcnt(0)\n\t" \
  "v_mfma_f32_16x16x32_bf16 v[76:79], v[60:63], v[216:219], v[76:79]\n\t" \
  "v_mfma_f32_16x16x32_bf16 v[92:95], v[60:63], v[220:223], v[92:95]\n\t" \
  "s_nop 7\n\t" \
  "s_nop 3\n\t"
#define OPROJ_CLOB "v32","v33","v34","v35","v36","v37","v38","v39","v40","v41","v42","v43","v44","v45","v46","v47","v48","v49","v50","v51","v52","v53","v54","v55","v56","v57","v58","v59","v60","v61","v62","v63","v96","v97","v98","v99","v100","v101","v102","v103","v104","v105","v106","v107","v108","v109","v110","v111","v112","v113","v114","v115","v116","v117","v118","v119","v120","v121","v122","v123","v124","v125","v126","v127","v128","v129","v130","v131","v132","v133","v134","v135","v136","v137","v138","v139","v140","v141","v142","v143","v144","v145","v146","v147","v148","v149","v150","v151","v152","v153","v154","v155","v156","v157","v158","v159","v160","v161","v162","v163","v164","v165","v166","v167","v168","v169","v170","v171","v172","v173","v174","v175","v176","v177","v178","v179","v180","v181","v182","v183","v184","v185","v186","v187","v188","v189","v190","v191","v192","v193","v194","v195","v196","v197","v198","v199","v200","v201","v202","v203","v204","v205","v206","v207","v208","v209","v210","v211","v212","v213","v214","v215","v216","v217","v218","v219","v220","v221","v222","v223","scc"
#define QKV_LOOP \
  "s_barrier\n\t" \
  "ds_read_b128 v[32:35], %[x0] offset:0\n\t" \
  "ds_read_b64_tr_b16 v[48:49], %[w0] offset:0\n\t" \
  "ds_read_b64_tr_b16 v[50:51], %[w0] offset:512\n\t" \
  "ds_read_b128 v[36:39], %[x0] offset:2048\n\t" \
  "ds_read_b128 v[40:43], %[x0] offset:4096\n\t" \
  "ds_read_b128 v[44:47], %[x0] offset:6144\n\t" \
  "ds_read_b64_tr_b16 v[52:53], %[w1] offset:0\n\t" \
  "ds_read_b64_tr_b16 v[54:55], %[w1] offset:512\n\t" \
  "ds_read_b64_tr_b16 v[56:57], %[w0] offset:8192\n\t" \
  "ds_read_b64_tr_b16 v[58:59], %[w0] offset:8704\n\t" \
  "ds_read_b64_tr_b16 v[60:61], %[w1] offset:8192\n\t" \
  "ds_read_b64_tr_b16 v[62:63], %[w1] offset:8704\n\t" \
  "ds_read_b64_tr_b16 v[64:65], %[w0] offset:16384\n\t" \
  "ds_read_b64_tr_b16 v[66:67], %[w0] offset:16896\n\t" \
  "ds_read_b64_tr_b16 v[68:69], %[w1] offset:16384\n\t" \
  "ds_read_b64_tr_b16 v[70:71], %[w1] offset:16896\n\t" \
  "s_cmp_eq_u32 %[sl], 2\n\t" \
  "s_cselect_b32 %[sd], %[cneg], %[cpos]\n\t" \
  "s_add_u32 %[sl], %[sl], 1\n\t" \
  "s_cmp_eq_u32 %[sl], 3\n\t" \
  "s_cselect_b32 %[sl], 0, %[sl]\n\t" \
  "s_waitcnt lgkmcnt(0)\n\t" \
  "v_mfma_f32_16x16x32_bf16 v[128:131], v[48:51], v[32:35], 0\n\t" \
  "ds_read_b128 v[72:75], %[x1] offset:0\n\t" \
  "v_mfma_f32_16x16x32_bf16 v[132:135], v[48:51], v[36:39], 0\n\t" \
  "ds_read_b64_tr_b16 v[88:89], %[w0] offset:4096\n\t" \
  "v_mfma_f32_16x16x32_bf16 v[136:139], v[48:51], v[40:43], 0\n\t" \
  "ds_read_b64_tr_b16 v[90:91], %[w0] offset:4608\n\t" \
  "v_mfma_f32_16x16x32_bf16 v[140:143], v[48:51], v[44:47], 0\n\t" \
  "ds_read_b128 v[76:79], %[x1] offset:2048\n\t" \
  "v_mfma_f32_16x16x32_bf16 v[156:159], v[52:55], v[44:47], 0\n\t" \
  "ds_read_b128 v[80:83], %[x1] offset:4096\n\t" \
  "v_mfma_f32_16x16x32_bf16 v[152:155], v[52:55], v[40:43], 0\n\t" \
  "ds_read_b128 v[84:87], %[x1] offset:6144\n\t" \
  "v_mfma_f32_16x16x32_bf16 v[148:151], v[52:55], v[36:39], 0\n\t" \
  "ds_read_b64_tr_b16 v[92:93], %[w1] offset:4096\n\t" \
  "v_mfma_f32_16x16x32_bf16 v[144:147], v[52:55], v[32:35], 0\n\t" \
  "ds_read_b64_tr_b16 v[94:95], %[w1] offset:4608\n\t" \
  "v_mfma_f32_16x16x32_bf16 v[160:163], v[56:59], v[32:35], 0\n\t" \
  "ds_read_b64_tr_b16 v[96:97], %[w0] offset:12288\n\t" \
  "v_mfma_f32_16x16x32_bf16 v[164:167], v[56:59], v[36:39], 0\n\t" \
  "ds_read_b64_tr_b16 v[98:99], %[w0] offset:12800\n\t" \
  "v_mfma_f32_16x16x32_bf16 v[168:171], v[56:59], v[40:43], 0\n\t" \
  "ds_read_b64_tr_b16 v[100:101], %[w1] offset:12288\n\t" \
  "v_mfma_f32_16x16x32_bf16 v[172:175], v[56:59], v[44:47], 0\n\t" \
  "ds_read_b64_tr_b16 v[102:103], %[w1] offset:12800\n\t" \
  "v_mfma_f32_16x16x32_bf16 v[188:191], v[60:63], v[44:47], 0\n\t" \
  "ds_read_b64_tr_b16 v[104:105], %[w0] offset:20480\n\t" \
  "v_mfma_f32_16x16x32_bf16 v[184:187], v[60:63], v[40:43], 0\n\t" \
  "ds_read_b64_tr_b16 v[106:107], %[w0] offset:20992\n\t" \
  "v_mfma_f32_16x16x32_bf16 v[180:183], v[60:63], v[36:39], 0\n\t" \
  "ds_read_b64_tr_b16 v[108:109], %[w1] offset:20480\n\t" \
  "v_mfma_f32_16x16x32_bf16 v[176:179], v[60:63], v[32:35], 0\n\t" \
  "ds_read_b64_tr_b16 v[110:111], %[w1] offset:20992\n\t" \
  "v_mfma_f32_16x16x32_bf16 v[192:195], v[32:35], v[64:67], 0\n\t" \
  "v_mfma_f32_16x16x32_bf16 v[196:199], v[36:39], v[64:67], 0\n\t" \
  "v_add_u32 %[x0], %[sd], %[x0]\n\t" \
  "v_mfma_f32_16x16x32_bf16 v[200:203], v[40:43], v[64:67], 0\n\t" \
  "v_add_u32 %[x1], %[sd], %[x1]\n\t" \
  "v_mfma_f32_16x16x32_bf16 v[204:207], v[44:47], v[64:67], 0\n\t" \
  "v_add_u32 %[w0], %[sd], %[w0]\n\t" \
  "v_mfma_f32_16x16x32_bf16 v[220:223], v[44:47], v[68:71], 0\n\t" \
  "v_add_u32 %[w1], %[sd], %[w1]\n\t" \
  "v_mfma_f32_16x16x32_bf16 v[216:219], v[40:43], v[68:71], 0\n\t" \
  "v_mfma_f32_16x16x32_bf16 v[212:215], v[36:39], v[68:71], 0\n\t" \
  "v_mfma_f32_16x16x32_bf16 v[208:211], v[32:35], v[68:71], 0\n\t" \
  "1:\n\t" \
  "s_cmp_lg_u32 %[cnt], 10\n\t" \
  "s_cbranch_scc1 2f\n\t" \
  "s_waitcnt vmcnt(0)\n\t" \
  "s_mov_b64 exec, 1\n\t" \
  "global_store_dwordx2 %[fo], %[tg], %[wsb] sc1\n\t" \
  "s_mov_b64 exec, -1\n\t" \
  "2:\n\t" \
  "s_waitcnt lgkmcnt(0)\n\t" \
  "s_barrier\n\t" \
  "v_mfma_f32_16x16x32_bf16 v[128:131], v[88:91], v[72:75], v[128:131]\n\t" \
  "ds_read_b128 v[32:35], %[x0] offset:0\n\t" \
  "v_mfma_f32_16x16x32_bf16 v[132:135], v[88:91], v[76:79], v[132:135]\n\t" \
  "ds_read_b64_tr_b16 v[48:49], %[w0] offset:0\n\t" \
  "v_mfma_f32_16x16x32_bf16 v[136:139], v[88:91], v[80:83], v[136:139]\n\t" \
  "ds_read_b64_tr_b16 v[50:51], %[w0] offset:512\n\t" \
  "v_mfma_f32_16x16x32_bf16 v[140:143], v[88:91], v[84:87], v[140:143]\n\t" \
  "ds_read_b128 v[36:39], %[x0] offset:2048\n\t" \
  "v_mfma_f32_16x16x32_bf16 v[156:159], v[92:95], v[84:87], v[156:159]\n\t" \
  "ds_read_b128 v[40:43], %[x0] offset:4096\n\t" \
  "v_mfma_f32_16x16x32_bf16 v[152:155], v[92:95], v[80:83], v[152:155]\n\t" \
  "ds_read_b128 v[44:47], %[x0] offset:6144\n\t" \
  "v_mfma_f32_16x16x32_bf16 v[148:151], v[92:95], v[76:79], v[148:151]\n\t" \
  "ds_read_b64_tr_b16 v[52:53], %[w1] offset:0\n\t" \
  "v_mfma_f32_16x16x32_bf16 v[144:147], v[92:95], v[72:75], v[144:147]\n\t" \
  "ds_read_b64_tr_b16 v[54:55], %[w1] offset:512\n\t" \
  "v_mfma_f32_16x16x32_bf16 v[160:163], v[96:99], v[72:75], v[160:163]\n\t" \
  "ds_read_b64_tr_b16 v[56:57], %[w0] offset:8192\n\t" \
  "v_mfma_f32_16x16x32_bf16 v[164:167], v[96:99], v[76:79], v[164:167]\n\t" \
  "ds_read_b64_tr_b16 v[58:59], %[w0] offset:8704\n\t" \
  "v_mfma_f32_16x16x32_bf16 v[168:171], v[96:99], v[80:83], v[168:171]\n\t" \
  "ds_read_b64_tr_b16 v[60:61], %[w1] offset:8192\n\t" \
  "v_mfma_f32_16x16x32_bf16 v[172:175], v[96:99], v[84:87], v[172:175]\n\t" \
  "ds_read_b64_tr_b16 v[62:63], %[w1] offset:8704\n\t" \
  "v_mfma_f32_16x16x32_bf16 v[188:191], v[100:103], v[84:87], v[188:191]\n\t" \
  "ds_read_b64_tr_b16 v[64:65], %[w0] offset:16384\n\t" \
  "v_mfma_f32_16x16x32_bf16 v[184:187], v[100:103], v[80:83], v[184:187]\n\t" \
  "ds_read_b64_tr_b16 v[66:67], %[w0] offset:16896\n\t" \
  "v_mfma_f32_16x16x32_bf16 v[180:183], v[100:103], v[76:79], v[180:183]\n\t" \
  "ds_read_b64_tr_b16 v[68:69], %[w1] offset:16384\n\t" \
  "v_mfma_f32_16x16x32_bf16 v[176:179], v[100:103], v[72:75], v[176:179]\n\t" \
  "ds_read_b64_tr_b16 v[70:71], %[w1] offset:16896\n\t" \
  "v_mfma_f32_16x16x32_bf16 v[192:195], v[72:75], v[104:107], v[192:195]\n\t" \
  "v_mfma_f32_16x16x32_bf16 v[196:199], v[76:79], v[104:107], v[196:199]\n\t" \
  "s_cmp_eq_u32 %[sl], 2\n\t" \
  "s_cselect_b32 %[sd], %[cneg], %[cpos]\n\t" \
  "s_add_u32 %[sl], %[sl], 1\n\t" \
  "s_cmp_eq_u32 %[sl], 3\n\t" \
  "s_cselect_b32 %[sl], 0, %[sl]\n\t" \
  "v_mfma_f32_16x16x32_bf16 v[200:203], v[80:83], v[104:107], v[200:203]\n\t" \
  "v_mfma_f32_16x16x32_bf16 v[204:207], v[84:87], v[104:107], v[204:207]\n\t" \
  "v_mfma_f32_16x16x32_bf16 v[220:223], v[84:87], v[108:111], v[220:223]\n\t" \
  "v_mfma_f32_16x16x32_bf16 v[216:219], v[80:83], v[108:111], v[216:219]\n\t" \
  "v_mfma_f32_16x16x32_bf16 v[212:215], v[76:79], v[108:111], v[212:215]\n\t" \
  "v_mfma_f32_16x16x32_bf16 v[208:211], v[72:75], v[108:111], v[208:211]\n\t" \
  "s_waitcnt lgkmcnt(0)\n\t" \
  "v_mfma_f32_16x16x32_bf16 v[128:131], v[48:51], v[32:35], v[128:131]\n\t" \
  "ds_read_b128 v[72:75], %[x1] offset:0\n\t" \
  "v_mfma_f32_16x16x32_bf16 v[132:135], v[48:51], v[36:39], v[132:135]\n\t" \
  "ds_read_b64_tr_b16 v[88:89], %[w0] offset:4096\n\t" \
  "v_mfma_f32_16x16x32_bf16 v[136:139], v[48:51], v[40:43], v[136:139]\n\t" \
  "ds_read_b64_tr_b16 v[90:91], %[w0] offset:4608\n\t" \
  "v_mfma_f32_16x16x32_bf16 v[140:143], v[48:51], v[44:47], v[140:143]\n\t" \
  "ds_read_b128 v[76:79], %[x1] offset:2048\n\t" \
  "v_mfma_f32_16x16x32_bf16 v[156:159], v[52:55], v[44:47], v[156:159]\n\t" \
  "ds_read_b128 v[80:83], %[x1] offset:4096\n\t" \
  "v_mfma_f32_16x16x32_bf16 v[152:155], v[52:55], v[40:43], v[152:155]\n\t" \
  "ds_read_b128 v[84:87], %[x1] offset:6144\n\t" \
  "v_mfma_f32_16x16x32_bf16 v[148:151], v[52:55], v[36:39], v[148:151]\n\t" \
  "ds_read_b64_tr_b16 v[92:93], %[w1] offset:4096\n\t" \
  "v_mfma_f32_16x16x32_bf16 v[144:147], v[52:55], v[32:35], v[144:147]\n\t" \
  "ds_read_b64_tr_b16 v[94:95], %[w1] offset:4608\n\t" \
  "v_mfma_f32_16x16x32_bf16 v[160:163], v[56:59], v[32:35], v[160:163]\n\t" \
  "ds_read_b64_tr_b16 v[96:97], %[w0] offset:12288\n\t" \
  "v_mfma_f32_16x16x32_bf16 v[164:167], v[56:59], v[36:39], v[164:167]\n\t" \
  "ds_read_b64_tr_b16 v[98:99], %[w0] offset:12800\n\t" \
  "v_mfma_f32_16x16x32_bf16 v[168:171], v[56:59], v[40:43], v[168:171]\n\t" \
  "ds_read_b64_tr_b16 v[100:101], %[w1] offset:12288\n\t" \
  "v_mfma_f32_16x16x32_bf16 v[172:175], v[56:59], v[44:47], v[172:175]\n\t" \
  "ds_read_b64_tr_b16 v[102:103], %[w1] offset:12800\n\t" \
  "v_mfma_f32_16x16x32_bf16 v[188:191], v[60:63], v[44:47], v[188:191]\n\t" \
  "ds_read_b64_tr_b16 v[104:105], %[w0] offset:20480\n\t" \
  "v_mfma_f32_16x16x32_bf16 v[184:187], v[60:63], v[40:43], v[184:187]\n\t" \
  "ds_read_b64_tr_b16 v[106:107], %[w0] offset:20992\n\t" \
  "v_mfma_f32_16x16x32_bf16 v[180:183], v[60:63], v[36:39], v[180:183]\n\t" \
  "ds_read_b64_tr_b16 v[108:109], %[w1] offset:20480\n\t" \
  "v_mfma_f32_16x16x32_bf16 v[176:179], v[60:63], v[32:35], v[176:179]\n\t" \
  "ds_read_b64_tr_b16 v[110:111], %[w1] offset:20992\n\t" \
  "v_mfma_f32_16x16x32_bf16 v[192:195], v[32:35], v[64:67], v[192:195]\n\t" \
  "v_mfma_f32_16x16x32_bf16 v[196:199], v[36:39], v[64:67], v[196:199]\n\t" \
  "v_add_u32 %[x0], %[sd], %[x0]\n\t" \
  "v_mfma_f32_16x16x32_bf16 v[200:203], v[40:43], v[64:67], v[200:203]\n\t" \
  "v_add_u32 %[x1], %[sd], %[x1]\n\t" \
  "v_mfma_f32_16x16x32_bf16 v[204:207], v[44:47], v[64:67], v[204:207]\n\t" \
  "v_add_u32 %[w0], %[sd], %[w0]\n\t" \
  "v_mfma_f32_16x16x32_bf16 v[220:223], v[44:47], v[68:71], v[220:223]\n\t" \
  "v_add_u32 %[w1], %[sd], %[w1]\n\t" \
  "v_mfma_f32_16x16x32_bf16 v[216:219], v[40:43], v[68:71], v[216:219]\n\t" \
  "s_sub_u32 %[cnt], %[cnt], 1\n\t" \
  "v_mfma_f32_16x16x32_bf16 v[212:215], v[36:39], v[68:71], v[212:215]\n\t" \
  "s_cmp_lg_u32 %[cnt], 0\n\t" \
  "v_mfma_f32_16x16x32_bf16 v[208:211], v[32:35], v[68:71], v[208:211]\n\t" \
  "s_cbranch_scc1 1b\n\t" \
  "s_waitcnt lgkmcnt(0)\n\t" \
  "s_barrier\n\t" \
  "v_mfma_f32_16x16x32_bf16 v[128:131], v[88:91], v[72:75], v[128:131]\n\t" \
  "ds_read_b128 v[32:35], %[x0] offset:0\n\t" \
  "v_mfma_f32_16x16x32_bf16 v[132:135], v[88:91], v[76:79], v[132:135]\n\t" \
  "ds_read_b64_tr_b16 v[56:57], %[w0] offset:8192\n\t" \
  "v_mfma_f32_16x16x32_bf16 v[136:139], v[88:91], v[80:83], v[136:139]\n\t" \
  "ds_read_b64_tr_b16 v[58:59], %[w0] offset:8704\n\t" \
  "v_mfma_f32_16x16x32_bf16 v[140:143], v[88:91], v[84:87], v[140:143]\n\t" \
  "ds_read_b128 v[36:39], %[x0] offset:2048\n\t" \
  "v_mfma_f32_16x16x32_bf16 v[156:159], v[92:95], v[84:87], v[156:159]\n\t" \
  "ds_read_b128 v[40:43], %[x0] offset:4096\n\t" \
  "v_mfma_f32_16x16x32_bf16 v[152:155], v[92:95], v[80:83], v[152:155]\n\t" \
  "ds_read_b128 v[44:47], %[x0] offset:6144\n\t" \
  "v_mfma_f32_16x16x32_bf16 v[148:151], v[92:95], v[76:79], v[148:151]\n\t" \
  "ds_read_b64_tr_b16 v[60:61], %[w1] offset:8192\n\t" \
  "v_mfma_f32_16x16x32_bf16 v[144:147], v[92:95], v[72:75], v[144:147]\n\t" \
  "ds_read_b64_tr_b16 v[62:63], %[w1] offset:8704\n\t" \
  "v_mfma_f32_16x16x32_bf16 v[160:163], v[96:99], v[72:75], v[160:163]\n\t" \
  "ds_read_b64_tr_b16 v[64:65], %[w0] offset:16384\n\t" \
  "v_mfma_f32_16x16x32_bf16 v[164:167], v[96:99], v[76:79], v[164:167]\n\t" \
  "ds_read_b64_tr_b16 v[66:67], %[w0] offset:16896\n\t" \
  "v_mfma_f32_16x16x32_bf16 v[168:171], v[96:99], v[80:83], v[168:171]\n\t" \
  "ds_read_b64_tr_b16 v[68:69], %[w1] offset:16384\n\t" \
  "v_mfma_f32_16x16x32_bf16 v[172:175], v[96:99], v[84:87], v[172:175]\n\t" \
  "ds_read_b64_tr_b16 v[70:71], %[w1] offset:16896\n\t" \
  "v_mfma_f32_16x16x32_bf16 v[188:191], v[100:103], v[84:87], v[188:191]\n\t" \
  "v_mfma_f32_16x16x32_bf16 v[184:187], v[100:103], v[80:83], v[184:187]\n\t" \
  "s_cmp_eq_u32 %[sl], 2\n\t" \
  "s_cselect_b32 %[sd], %[cneg], %[cpos]\n\t" \
  "s_add_u32 %[sl], %[sl], 1\n\t" \
  "s_cmp_eq_u32 %[sl], 3\n\t" \
  "s_cselect_b32 %[sl], 0, %[sl]\n\t" \
  "v_mfma_f32_16x16x32_bf16 v[180:183], v[100:103], v[76:79], v[180:183]\n\t" \
  "v_mfma_f32_16x16x32_bf16 v[176:179], v[100:103], v[72:75], v[176:179]\n\t" \
  "v_mfma_f32_16x16x32_bf16 v[192:195], v[72:75], v[104:107], v[192:195]\n\t" \
  "v_mfma_f32_16x16x32_bf16 v[196:199], v[76:79], v[104:107], v[196:199]\n\t" \
  "v_mfma_f32_16x16x32_bf16 v[200:203], v[80:83], v[104:107], v[200:203]\n\t" \
  "v_mfma_f32_16x16x32_bf16 v[204:207], v[84:87], v[104:107], v[204:207]\n\t" \
  "v_mfma_f32_16x16x32_bf16 v[220:223], v[84:87], v[108:111], v[220:223]\n\t" \
  "v_mfma_f32_16x16x32_bf16 v[216:219], v[80:83], v[108:111], v[216:219]\n\t" \
  "v_mfma_f32_16x16x32_bf16 v[212:215], v[76:79], v[108:111], v[212:215]\n\t" \
  "v_mfma_f32_16x16x32_bf16 v[208:211], v[72:75], v[108:111], v[208:211]\n\t" \
  "s_waitcnt lgkmcnt(0)\n\t" \
  "v_mfma_f32_16x16x32_bf16 v[160:163], v[56:59], v[32:35], v[160:163]\n\t" \
  "ds_read_b128 v[72:75], %[x1] offset:0\n\t" \
  "v_mfma_f32_16x16x32_bf16 v[164:167], v[56:59], v[36:39], v[164:167]\n\t" \
  "ds_read_b64_tr_b16 v[96:97], %[w0] offset:12288\n\t" \
  "v_mfma_f32_16x16x32_bf16 v[168:171], v[56:59], v[40:43], v[168:171]\n\t" \
  "ds_read_b64_tr_b16 v[98:99], %[w0] offset:12800\n\t" \
  "v_mfma_f32_16x16x32_bf16 v[172:175], v[56:59], v[44:47], v[172:175]\n\t" \
  "ds_read_b128 v[76:79], %[x1] offset:2048\n\t" \
  "v_mfma_f32_16x16x32_bf16 v[188:191], v[60:63], v[44:47], v[188:191]\n\t" \
  "ds_read_b128 v[80:83], %[x1] offset:4096\n\t" \
  "v_mfma_f32_16x16x32_bf16 v[184:187], v[60:63], v[40:43], v[184:187]\n\t" \
  "ds_read_b128 v[84:87], %[x1] offset:6144\n\t" \
  "v_mfma_f32_16x16x32_bf16 v[180:183], v[60:63], v[36:39], v[180:183]\n\t" \
  "ds_read_b64_tr_b16 v[100:101], %[w1] offset:12288\n\t" \
  "v_mfma_f32_16x16x32_bf16 v[176:179], v[60:63], v[32:35], v[176:179]\n\t" \
  "ds_read_b64_tr_b16 v[102:103], %[w1] offset:12800\n\t" \
  "v_mfma_f32_16x16x32_bf16 v[192:195], v[32:35], v[64:67], v[192:195]\n\t" \
  "ds_read_b64_tr_b16 v[104:105], %[w0] offset:20480\n\t" \
  "v_mfma_f32_16x16x32_bf16 v[196:199], v[36:39], v[64:67], v[196:199]\n\t" \
  "ds_read_b64_tr_b16 v[106:107], %[w0] offset:20992\n\t" \
  "v_mfma_f32_16x16x32_bf16 v[200:203], v[40:43], v[64:67], v[200:203]\n\t" \
  "ds_read_b64_tr_b16 v[108:109], %[w1] offset:20480\n\t" \
  "v_mfma_f32_16x16x32_bf16 v[204:207], v[44:47], v[64:67], v[204:207]\n\t" \
  "ds_read_b64_tr_b16 v[110:111], %[w1] offset:20992\n\t" \
  "v_mfma_f32_16x16x32_bf16 v[220:223], v[44:47], v[68:71], v[220:223]\n\t" \
  "v_add_u32 %[x0], %[sd], %[x0]\n\t" \
  "v_mfma_f32_16x16x32_bf16 v[216:219], v[40:43], v[68:71], v[216:219]\n\t" \
  "v_add_u32 %[x1], %[sd], %[x1]\n\t" \
  "v_mfma_f32_16x16x32_bf16 v[212:215], v[36:39], v[68:71], v[212:215]\n\t" \
  "v_add_u32 %[w0], %[sd], %[w0]\n\t" \
  "v_mfma_f32_16x16x32_bf16 v[208:211], v[32:35], v[68:71], v[208:211]\n\t" \
  "v_add_u32 %[w1], %[sd], %[w1]\n\t" \
  "s_waitcnt lgkmcnt(0)\n\t" \
  "s_barrier\n\t" \
  "v_mfma_f32_16x16x32_bf16 v[160:163], v[96:99], v[72:75], v[160:163]\n\t" \
  "ds_read_b128 v[32:35], %[x0] offset:0\n\t" \
  "v_mfma_f32_16x16x32_bf16 v[164:167], v[96:99], v[76:79], v[164:167]\n\t" \
  "ds_read_b64_tr_b16 v[56:57], %[w0] offset:8192\n\t" \
  "v_mfma_f32_16x16x32_bf16 v[168:171], v[96:99], v[80:83], v[168:171]\n\t" \
  "ds_read_b64_tr_b16 v[58:59], %[w0] offset:8704\n\t" \
  "v_mfma_f32_16x16x32_bf16 v[172:175], v[96:99], v[84:87], v[172:175]\n\t" \
  "ds_read_b128 v[36:39], %[x0] offset:2048\n\t" \
  "v_mfma_f32_16x16x32_bf16 v[188:191], v[100:103], v[84:87], v[188:191]\n\t" \
  "ds_read_b128 v[40:43], %[x0] offset:4096\n\t" \
  "v_mfma_f32_16x16x32_bf16 v[184:187], v[100:103], v[80:83], v[184:187]\n\t" \
  "ds_read_b128 v[44:47], %[x0] offset:6144\n\t" \
  "v_mfma_f32_16x16x32_bf16 v[180:183], v[100:103], v[76:79], v[180:183]\n\t" \
  "ds_read_b64_tr_b16 v[60:61], %[w1] offset:8192\n\t" \
  "v_mfma_f32_16x16x32_bf16 v[176:179], v[100:103], v[72:75], v[176:179]\n\t" \
  "ds_read_b64_tr_b16 v[62:63], %[w1] offset:8704\n\t" \
  "v_mfma_f32_16x16x32_bf16 v[192:195], v[72:75], v[104:107], v[192:195]\n\t" \
  "ds_read_b64_tr_b16 v[64:65], %[w0] offset:16384\n\t" \
  "v_mfma_f32_16x16x32_bf16 v[196:199], v[76:79], v[104:107], v[196:199]\n\t" \
  "ds_read_b64_tr_b16 v[66:67], %[w0] offset:16896\n\t" \
  "v_mfma_f32_16x16x32_bf16 v[200:203], v[80:83], v[104:107], v[200:203]\n\t" \
  "ds_read_b64_tr_b16 v[68:69], %[w1] offset:16384\n\t" \
  "v_mfma_f32_16x16x32_bf16 v[204:207], v[84:87], v[104:107], v[204:207]\n\t" \
  "ds_read_b64_tr_b16 v[70:71], %[w1] offset:16896\n\t" \
  "v_mfma_f32_16x16x32_bf16 v[220:223], v[84:87], v[108:111], v[220:223]\n\t" \
  "v_mfma_f32_16x16x32_bf16 v[216:219], v[80:83], v[108:111], v[216:219]\n\t" \
  "s_cmp_eq_u32 %[sl], 2\n\t" \
  "s_cselect_b32 %[sd], %[cneg], %[cpos]\n\t" \
  "s_add_u32 %[sl], %[sl], 1\n\t" \
  "s_cmp_eq_u32 %[sl], 3\n\t" \
  "s_cselect_b32 %[sl], 0, %[sl]\n\t" \
  "v_mfma_f32_16x16x32_bf16 v[212:215], v[76:79], v[108:111], v[212:215]\n\t" \
  "v_mfma_f32_16x16x32_bf16 v[208:211], v[72:75], v[108:111], v[208:211]\n\t" \
  "s_waitcnt lgkmcnt(0)\n\t" \
  "v_mfma_f32_16x16x32_bf16 v[160:163], v[56:59], v[32:35], v[160:163]\n\t" \
  "ds_read_b128 v[72:75], %[x1] offset:0\n\t" \
  "v_mfma_f32_16x16x32_bf16 v[164:167], v[56:59], v[36:39], v[164:167]\n\t" \
  "ds_read_b64_tr_b16 v[96:97], %[w0] offset:12288\n\t" \
  "v_mfma_f32_16x16x32_bf16 v[168:171], v[56:59], v[40:43], v[168:171]\n\t" \
  "ds_read_b64_tr_b16 v[98:99], %[w0] offset:12800\n\t" \
  "v_mfma_f32_16x16x32_bf16 v[172:175], v[56:59], v[44:47], v[172:175]\n\t" \
  "ds_read_b128 v[76:79], %[x1] offset:2048\n\t" \
  "v_mfma_f32_16x16x32_bf16 v[188:191], v[60:63], v[44:47], v[188:191]\n\t" \
  "ds_read_b128 v[80:83], %[x1] offset:4096\n\t" \
  "v_mfma_f32_16x16x32_bf16 v[184:187], v[60:63], v[40:43], v[184:187]\n\t" \
  "ds_read_b128 v[84:87], %[x1] offset:6144\n\t" \
  "v_mfma_f32_16x16x32_bf16 v[180:183], v[60:63], v[36:39], v[180:183]\n\t" \
  "ds_read_b64_tr_b16 v[100:101], %[w1] offset:12288\n\t" \
  "v_mfma_f32_16x16x32_bf16 v[176:179], v[60:63], v[32:35], v[176:179]\n\t" \
  "ds_read_b64_tr_b16 v[102:103], %[w1] offset:12800\n\t" \
  "v_mfma_f32_16x16x32_bf16 v[192:195], v[32:35], v[64:67], v[192:195]\n\t" \
  "ds_read_b64_tr_b16 v[104:105], %[w0] offset:20480\n\t" \
  "v_mfma_f32_16x16x32_bf16 v[196:199], v[36:39], v[64:67], v[196:199]\n\t" \
  "ds_read_b64_tr_b16 v[106:107], %[w0] offset:20992\n\t" \
  "v_mfma_f32_16x16x32_bf16 v[200:203], v[40:43], v[64:67], v[200:203]\n\t" \
  "ds_read_b64_tr_b16 v[108:109], %[w1] offset:20480\n\t" \
  "v_mfma_f32_16x16x32_bf16 v[204:207], v[44:47], v[64:67], v[204:207]\n\t" \
  "ds_read_b64_tr_b16 v[110:111], %[w1] offset:20992\n\t" \
  "v_mfma_f32_16x16x32_bf16 v[220:223], v[44:47], v[68:71], v[220:223]\n\t" \
  "v_add_u32 %[x0], %[sd], %[x0]\n\t" \
  "v_mfma_f32_16x16x32_bf16 v[216:219], v[40:43], v[68:71], v[216:219]\n\t" \
  "v_add_u32 %[x1], %[sd], %[x1]\n\t" \
  "v_mfma_f32_16x16x32_bf16 v[212:215], v[36:39], v[68:71], v[212:215]\n\t" \
  "v_add_u32 %[w0], %[sd], %[w0]\n\t" \
  "v_mfma_f32_16x16x32_bf16 v[208:211], v[32:35], v[68:71], v[208:211]\n\t" \
  "v_add_u32 %[w1], %[sd], %[w1]\n\t" \
  "s_waitcnt lgkmcnt(0)\n\t" \
  "s_barrier\n\t" \
  "v_mfma_f32_16x16x32_bf16 v[160:163], v[96:99], v[72:75], v[160:163]\n\t" \
  "ds_read_b128 v[32:35], %[x0] offset:0\n\t" \
  "v_mfma_f32_16x16x32_bf16 v[164:167], v[96:99], v[76:79], v[164:167]\n\t" \
  "ds_read_b64_tr_b16 v[56:57], %[w0] offset:8192\n\t" \
  "v_mfma_f32_16x16x32_bf16 v[168:171], v[96:99], v[80:83], v[168:171]\n\t" \
  "ds_read_b64_tr_b16 v[58:59], %[w0] offset:8704\n\t" \
  "v_mfma_f32_16x16x32_bf16 v[172:175], v[96:99], v[84:87], v[172:175]\n\t" \
  "ds_read_b128 v[36:39], %[x0] offset:2048\n\t" \
  "v_mfma_f32_16x16x32_bf16 v[188:191], v[100:103], v[84:87], v[188:191]\n\t" \
  "ds_read_b128 v[40:43], %[x0] offset:4096\n\t" \
  "v_mfma_f32_16x16x32_bf16 v[184:187], v[100:103], v[80:83], v[184:187]\n\t" \
  "ds_read_b128 v[44:47], %[x0] offset:6144\n\t" \
  "v_mfma_f32_16x16x32_bf16 v[180:183], v[100:103], v[76:79], v[180:183]\n\t" \
  "ds_read_b64_tr_b16 v[60:61], %[w1] offset:8192\n\t" \
  "v_mfma_f32_16x16x32_bf16 v[176:179], v[100:103], v[72:75], v[176:179]\n\t" \
  "ds_read_b64_tr_b16 v[62:63], %[w1] offset:8704\n\t" \
  "v_mfma_f32_16x16x32_bf16 v[192:195], v[72:75], v[104:107], v[192:195]\n\t" \
  "ds_read_b64_tr_b16 v[64:65], %[w0] offset:16384\n\t" \
  "v_mfma_f32_16x16x32_bf16 v[196:199], v[76:79], v[104:107], v[196:199]\n\t" \
  "ds_read_b64_tr_b16 v[66:67], %[w0] offset:16896\n\t" \
  "v_mfma_f32_16x16x32_bf16 v[200:203], v[80:83], v[104:107], v[200:203]\n\t" \
  "ds_read_b64_tr_b16 v[68:69], %[w1] offset:16384\n\t" \
  "v_mfma_f32_16x16x32_bf16 v[204:207], v[84:87], v[104:107], v[204:207]\n\t" \
  "ds_read_b64_tr_b16 v[70:71], %[w1] offset:16896\n\t" \
  "v_mfma_f32_16x16x32_bf16 v[220:223], v[84:87], v[108:111], v[220:223]\n\t" \
  "v_mfma_f32_16x16x32_bf16 v[216:219], v[80:83], v[108:111], v[216:219]\n\t" \
  "v_mfma_f32_16x16x32_bf16 v[212:215], v[76:79], v[108:111], v[212:215]\n\t" \
  "v_mfma_f32_16x16x32_bf16 v[208:211], v[72:75], v[108:111], v[208:211]\n\t" \
  "s_waitcnt lgkmcnt(0)\n\t" \
  "v_mfma_f32_16x16x32_bf16 v[160:163], v[56:59], v[32:35], v[160:163]\n\t" \
  "ds_read_b128 v[72:75], %[x1] offset:0\n\t" \
  "v_mfma_f32_16x16x32_bf16 v[164:167], v[56:59], v[36:39], v[164:167]\n\t" \
  "ds_read_b64_tr_b16 v[96:97], %[w0] offset:12288\n\t" \
  "v_mfma_f32_16x16x32_bf16 v[168:171], v[56:59], v[40:43], v[168:171]\n\t" \
  "ds_read_b64_tr_b16 v[98:99], %[w0] offset:12800\n\t" \
  "v_mfma_f32_16x16x32_bf16 v[172:175], v[56:59], v[44:47], v[172:175]\n\t" \
  "ds_read_b128 v[76:79], %[x1] offset:2048\n\t" \
  "v_mfma_f32_16x16x32_bf16 v[188:191], v[60:63], v[44:47], v[188:191]\n\t" \
  "ds_read_b128 v[80:83], %[x1] offset:4096\n\t" \
  "v_mfma_f32_16x16x32_bf16 v[184:187], v[60:63], v[40:43], v[184:187]\n\t" \
  "ds_read_b128 v[84:87], %[x1] offset:6144\n\t" \
  "v_mfma_f32_16x16x32_bf16 v[180:183], v[60:63], v[36:39], v[180:183]\n\t" \
  "ds_read_b64_tr_b16 v[100:101], %[w1] offset:12288\n\t" \
  "v_mfma_f32_16x16x32_bf16 v[176:179], v[60:63], v[32:35], v[176:179]\n\t" \
  "ds_read_b64_tr_b16 v[102:103], %[w1] offset:12800\n\t" \
  "v_mfma_f32_16x16x32_bf16 v[192:195], v[32:35], v[64:67], v[192:195]\n\t" \
  "ds_read_b64_tr_b16 v[104:105], %[w0] offset:20480\n\t" \
  "v_mfma_f32_16x16x32_bf16 v[196:199], v[36:39], v[64:67], v[196:199]\n\t" \
  "ds_read_b64_tr_b16 v[106:107], %[w0] offset:20992\n\t" \
  "v_mfma_f32_16x16x32_bf16 v[200:203], v[40:43], v[64:67], v[200:203]\n\t" \
  "ds_read_b64_tr_b16 v[108:109], %[w1] offset:20480\n\t" \
  "v_mfma_f32_16x16x32_bf16 v[204:207], v[44:47], v[64:67], v[204:207]\n\t" \
  "ds_read_b64_tr_b16 v[110:111], %[w1] offset:20992\n\t" \
  "v_mfma_f32_16x16x32_bf16 v[220:223], v[44:47], v[68:71], v[220:223]\n\t" \
  "v_mfma_f32_16x16x32_bf16 v[216:219], v[40:43], v[68:71], v[216:219]\n\t" \
  "v_mfma_f32_16x16x32_bf16 v[212:215], v[36:39], v[68:71], v[212:215]\n\t" \
  "v_mfma_f32_16x16x32_bf16 v[208:211], v[32:35], v[68:71], v[208:211]\n\t" \
  "s_waitcnt lgkmcnt(0)\n\t" \
  "v_mfma_f32_16x16x32_bf16 v[160:163], v[96:99], v[72:75], v[160:163]\n\t" \
  "ds_read_b128 v[32:35], %[x0] offset:0\n\t" \
  "v_mfma_f32_16x16x32_bf16 v[164:167], v[96:99], v[76:79], v[164:167]\n\t" \
  "ds_read_b64_tr_b16 v[48:49], %[w0] offset:0\n\t" \
  "v_mfma_f32_16x16x32_bf16 v[168:171], v[96:99], v[80:83], v[168:171]\n\t" \
  "ds_read_b64_tr_b16 v[50:51], %[w0] offset:512\n\t" \
  "v_mfma_f32_16x16x32_bf16 v[172:175], v[96:99], v[84:87], v[172:175]\n\t" \
  "ds_read_b128 v[36:39], %[x0] offset:2048\n\t" \
  "v_mfma_f32_16x16x32_bf16 v[188:191], v[100:103], v[84:87], v[188:191]\n\t" \
  "ds_read_b128 v[40:43], %[x0] offset:4096\n\t" \
  "v_mfma_f32_16x16x32_bf16 v[184:187], v[100:103], v[80:83], v[184:187]\n\t" \
  "ds_read_b128 v[44:47], %[x0] offset:6144\n\t" \
  "v_mfma_f32_16x16x32_bf16 v[180:183], v[100:103], v[76:79], v[180:183]\n\t" \
  "ds_read_b64_tr_b16 v[52:53], %[w1] offset:0\n\t" \
  "v_mfma_f32_16x16x32_bf16 v[176:179], v[100:103], v[72:75], v[176:179]\n\t" \
  "ds_read_b64_tr_b16 v[54:55], %[w1] offset:512\n\t" \
  "v_mfma_f32_16x16x32_bf16 v[192:195], v[72:75], v[104:107], v[192:195]\n\t" \
  "v_mfma_f32_16x16x32_bf16 v[196:199], v[76:79], v[104:107], v[196:199]\n\t" \
  "v_mfma_f32_16x16x32_bf16 v[200:203], v[80:83], v[104:107], v[200:203]\n\t" \
  "v_mfma_f32_16x16x32_bf16 v[204:207], v[84:87], v[104:107], v[204:207]\n\t" \
  "v_mfma_f32_16x16x32_bf16 v[220:223], v[84:87], v[108:111], v[220:223]\n\t" \
  "v_mfma_f32_16x16x32_bf16 v[216:219], v[80:83], v[108:111], v[216:219]\n\t" \
  "v_mfma_f32_16x16x32_bf16 v[212:215], v[76:79], v[108:111], v[212:215]\n\t" \
  "v_mfma_f32_16x16x32_bf16 v[208:211], v[72:75], v[108:111], v[208:211]\n\t" \
  "ds_read_b128 v[56:59], %[x1] offset:0\n\t" \
  "ds_read_b64_tr_b16 v[72:73], %[w0] offset:4096\n\t" \
  "ds_read_b64_tr_b16 v[74:75], %[w0] offset:4608\n\t" \
  "ds_read_b128 v[60:63], %[x1] offset:2048\n\t" \
  "ds_read_b128 v[64:67], %[x1] offset:4096\n\t" \
  "ds_read_b128 v[68:71], %[x1] offset:6144\n\t" \
  "ds_read_b64_tr_b16 v[76:77], %[w1] offset:4096\n\t" \
  "ds_read_b64_tr_b16 v[78:79], %[w1] offset:4608\n\t" \
  "s_waitcnt lgkmcnt(8)\n\t" \
  "v_mfma_f32_16x16x32_bf16 v[128:131], v[48:51], v[32:35], v[128:131]\n\t" \
  "ds_read_b128 v[80:83], %[x0] offset:40960\n\t" \
  "v_cvt_pk_bf16_f32 v112, v160, v161\n\t" \
  "v_mfma_f32_16x16x32_bf16 v[132:135], v[48:51], v[36:39], v[132:135]\n\t" \
  "ds_read_b64_tr_b16 v[96:97], %[w0] offset:40960\n\t" \
  "v_cvt_pk_bf16_f32 v113, v162, v163\n\t" \
  "v_mfma_f32_16x16x32_bf16 v[136:139], v[48:51], v[40:43], v[136:139]\n\t" \
  "ds_read_b64_tr_b16 v[98:99], %[w0] offset:41472\n\t" \
  "v_cvt_pk_bf16_f32 v114, v176, v177\n\t" \
  "v_mfma_f32_16x16x32_bf16 v[140:143], v[48:51], v[44:47], v[140:143]\n\t" \
  "ds_read_b128 v[84:87], %[x0] offset:43008\n\t" \
  "v_cvt_pk_bf16_f32 v115, v178, v179\n\t" \
  "v_mfma_f32_16x16x32_bf16 v[156:159], v[52:55], v[44:47], v[156:159]\n\t" \
  "ds_read_b128 v[88:91], %[x0] offset:45056\n\t" \
  "buffer_store_dwordx4 v[112:115], %[g0], %[rs], 0 offen offset:0 sc1\n\t" \
  "v_mfma_f32_16x16x32_bf16 v[152:155], v[52:55], v[40:43], v[152:155]\n\t" \
  "ds_read_b128 v[92:95], %[x0] offset:47104\n\t" \
  "v_cvt_pk_bf16_f32 v116, v164, v165\n\t" \
  "v_mfma_f32_16x16x32_bf16 v[148:151], v[52:55], v[36:39], v[148:151]\n\t" \
  "ds_read_b64_tr_b16 v[100:101], %[w1] offset:40960\n\t" \
  "v_cvt_pk_bf16_f32 v117, v166, v167\n\t" \
  "v_mfma_f32_16x16x32_bf16 v[144:147], v[52:55], v[32:35], v[144:147]\n\t" \
  "ds_read_b64_tr_b16 v[102:103], %[w1] offset:41472\n\t" \
  "v_cvt_pk_bf16_f32 v118, v180, v181\n\t" \
  "s_waitcnt lgkmcnt(8)\n\t" \
  "v_mfma_f32_16x16x32_bf16 v[128:131], v[72:75], v[56:59], v[128:131]\n\t" \
  "ds_read_b128 v[32:35], %[x1] offset:40960\n\t" \
  "v_cvt_pk_bf16_f32 v119, v182, v183\n\t" \
  "v_mfma_f32_16x16x32_bf16 v[132:135], v[72:75], v[60:63], v[132:135]\n\t" \
  "ds_read_b64_tr_b16 v[48:49], %[w0] offset:45056\n\t" \
  "buffer_store_dwordx4 v[116:119], %[g0], %[rs], 0 offen offset:1024 sc1\n\t" \
  "v_mfma_f32_16x16x32_bf16 v[136:139], v[72:75], v[64:67], v[136:139]\n\t" \
  "ds_read_b64_tr_b16 v[50:51], %[w0] offset:45568\n\t" \
  "v_cvt_pk_bf16_f32 v120, v168, v169\n\t" \
  "v_mfma_f32_16x16x32_bf16 v[140:143], v[72:75], v[68:71], v[140:143]\n\t" \
  "ds_read_b128 v[36:39], %[x1] offset:43008\n\t" \
  "v_cvt_pk_bf16_f32 v121, v170, v171\n\t" \
  "v_mfma_f32_16x16x32_bf16 v[156:159], v[76:79], v[68:71], v[156:159]\n\t" \
  "ds_read_b128 v[40:43], %[x1] offset:45056\n\t" \
  "v_cvt_pk_bf16_f32 v122, v184, v185\n\t" \
  "v_mfma_f32_16x16x32_bf16 v[152:155], v[76:79], v[64:67], v[152:155]\n\t" \
  "ds_read_b128 v[44:47], %[x1] offset:47104\n\t" \
  "v_cvt_pk_bf16_f32 v123, v186, v187\n\t" \
  "v_mfma_f32_16x16x32_bf16 v[148:151], v[76:79], v[60:63], v[148:151]\n\t" \
  "ds_read_b64_tr_b16 v[52:53], %[w1] offset:45056\n\t" \
  "buffer_store_dwordx4 v[120:123], %[g1], %[rs], 0 offen offset:0 sc1\n\t" \
  "v_mfma_f32_16x16x32_bf16 v[144:147], v[76:79], v[56:59], v[144:147]\n\t" \
  "ds_read_b64_tr_b16 v[54:55], %[w1] offset:45568\n\t" \
  "v_cvt_pk_bf16_f32 v124, v172, v173\n\t" \
  "s_waitcnt lgkmcnt(8)\n\t" \
  "v_add_u32 %[x0], 0x14000, %[x0]\n\t" \
  "v_add_u32 %[x1], 0x14000, %[x1]\n\t" \
  "v_add_u32 %[w0], 0x14000, %[w0]\n\t" \
  "v_add_u32 %[w1], 0x14000, %[w1]\n\t" \
  "v_mfma_f32_16x16x32_bf16 v[128:131], v[96:99], v[80:83], v[128:131]\n\t" \
  "ds_read_b128 v[56:59], %[x0] offset:0\n\t" \
  "v_cvt_pk_bf16_f32 v125, v174, v175\n\t" \
  "v_mfma_f32_16x16x32_bf16 v[132:135], v[96:99], v[84:87], v[132:135]\n\t" \
  "ds_read_b64_tr_b16 v[72:73], %[w0] offset:0\n\t" \
  "v_cvt_pk_bf16_f32 v126, v188, v189\n\t" \
  "v_mfma_f32_16x16x32_bf16 v[136:139], v[96:99], v[88:91], v[136:139]\n\t" \
  "ds_read_b64_tr_b16 v[74:75], %[w0] offset:512\n\t" \
  "v_cvt_pk_bf16_f32 v127, v190, v191\n\t" \
  "v_mfma_f32_16x16x32_bf16 v[140:143], v[96:99], v[92:95], v[140:143]\n\t" \
  "ds_read_b128 v[60:63], %[x0] offset:2048\n\t" \
  "buffer_store_dwordx4 v[124:127], %[g1], %[rs], 0 offen offset:1024 sc1\n\t" \
  "v_mfma_f32_16x16x32_bf16 v[156:159], v[100:103], v[92:95], v[156:159]\n\t" \
  "ds_read_b128 v[64:67], %[x0] offset:4096\n\t" \
  "v_cvt_pk_bf16_f32 v160, v192, v193\n\t" \
  "v_mfma_f32_16x16x32_bf16 v[152:155], v[100:103], v[88:91], v[152:155]\n\t" \
  "ds_read_b128 v[68:71], %[x0] offset:6144\n\t" \
  "v_cvt_pk_bf16_f32 v161, v194, v195\n\t" \
  "v_mfma_f32_16x16x32_bf16 v[148:151], v[100:103], v[84:87], v[148:151]\n\t" \
  "ds_read_b64_tr_b16 v[76:77], %[w1] offset:0\n\t" \
  "v_cvt_pk_bf16_f32 v162, v196, v197\n\t" \
  "v_mfma_f32_16x16x32_bf16 v[144:147], v[100:103], v[80:83], v[144:147]\n\t" \
  "ds_read_b64_tr_b16 v[78:79], %[w1] offset:512\n\t" \
  "v_cvt_pk_bf16_f32 v163, v198, v199\n\t" \
  "s_waitcnt lgkmcnt(8)\n\t" \
  "v_mfma_f32_16x16x32_bf16 v[128:131], v[48:51], v[32:35], v[128:131]\n\t" \
  "ds_read_b128 v[80:83], %[x1] offset:0\n\t" \
  "buffer_store_dwordx4 v[160:163], %[g0], %[rs], 0 offen offset:2048 sc1\n\t" \
  "v_mfma_f32_16x16x32_bf16 v[132:135], v[48:51], v[36:39], v[132:135]\n\t" \
  "ds_read_b64_tr_b16 v[96:97], %[w0] offset:4096\n\t" \
  "v_cvt_pk_bf16_f32 v164, v208, v209\n\t" \
  "v_mfma_f32_16x16x32_bf16 v[136:139], v[48:51], v[40:43], v[136:139]\n\t" \
  "ds_read_b64_tr_b16 v[98:99], %[w0] offset:4608\n\t" \
  "v_cvt_pk_bf16_f32 v165, v210, v211\n\t" \
  "v_mfma_f32_16x16x32_bf16 v[140:143], v[48:51], v[44:47], v[140:143]\n\t" \
  "ds_read_b128 v[84:87], %[x1] offset:2048\n\t" \
  "v_cvt_pk_bf16_f32 v166, v212, v213\n\t" \
  "v_mfma_f32_16x16x32_bf16 v[156:159], v[52:55], v[44:47], v[156:159]\n\t" \
  "ds_read_b128 v[88:91], %[x1] offset:4096\n\t" \
  "v_cvt_pk_bf16_f32 v167, v214, v215\n\t" \
  "v_mfma_f32_16x16x32_bf16 v[152:155], v[52:55], v[40:43], v[152:155]\n\t" \
  "ds_read_b128 v[92:95], %[x1] offset:6144\n\t" \
  "buffer_store_dwordx4 v[164:167], %[g0], %[rs], 0 offen offset:3072 sc1\n\t" \
  "v_mfma_f32_16x16x32_bf16 v[148:151], v[52:55], v[36:39], v[148:151]\n\t" \
  "ds_read_b64_tr_b16 v[100:101], %[w1] offset:4096\n\t" \
  "v_cvt_pk_bf16_f32 v168, v200, v201\n\t" \
  "v_mfma_f32_16x16x32_bf16 v[144:147], v[52:55], v[32:35], v[144:147]\n\t" \
  "ds_read_b64_tr_b16 v[102:103], %[w1] offset:4608\n\t" \
  "v_cvt_pk_bf16_f32 v169, v202, v203\n\t" \
  "s_waitcnt lgkmcnt(8)\n\t" \
  "v_mfma_f32_16x16x32_bf16 v[128:131], v[72:75], v[56:59], v[128:131]\n\t" \
  "v_cvt_pk_bf16_f32 v170, v204, v205\n\t" \
  "v_mfma_f32_16x16x32_bf16 v[132:135], v[72:75], v[60:63], v[132:135]\n\t" \
  "v_cvt_pk_bf16_f32 v171, v206, v207\n\t" \
  "v_mfma_f32_16x16x32_bf16 v[136:139], v[72:75], v[64:67], v[136:139]\n\t" \
  "buffer_store_dwordx4 v[168:171], %[g1], %[rs], 0 offen offset:2048 sc1\n\t" \
  "v_mfma_f32_16x16x32_bf16 v[140:143], v[72:75], v[68:71], v[140:143]\n\t" \
  "v_cvt_pk_bf16_f32 v172, v216, v217\n\t" \
  "v_mfma_f32_16x16x32_bf16 v[156:159], v[76:79], v[68:71], v[156:159]\n\t" \
  "v_cvt_pk_bf16_f32 v173, v218, v219\n\t" \
  "v_mfma_f32_16x16x32_bf16 v[152:155], v[76:79], v[64:67], v[152:155]\n\t" \
  "v_cvt_pk_bf16_f32 v174, v220, v221\n\t" \
  "v_mfma_f32_16x16x32_bf16 v[148:151], v[76:79], v[60:63], v[148:151]\n\t" \
  "v_cvt_pk_bf16_f32 v175, v222, v223\n\t" \
  "v_mfma_f32_16x16x32_bf16 v[144:147], v[76:79], v[56:59], v[144:147]\n\t" \
  "buffer_store_dwordx4 v[172:175], %[g1], %[rs], 0 offen offset:3072 sc1\n\t" \
  "s_waitcnt lgkmcnt(0)\n\t" \
  "s_barrier\n\t" \
  "v_mfma_f32_16x16x32_bf16 v[128:131], v[96:99], v[80:83], v[128:131]\n\t" \
  "v_mfma_f32_16x16x32_bf16 v[132:135], v[96:99], v[84:87], v[132:135]\n\t" \
  "v_mfma_f32_16x16x32_bf16 v[136:139], v[96:99], v[88:91], v[136:139]\n\t" \
  "v_mfma_f32_16x16x32_bf16 v[140:143], v[96:99], v[92:95], v[140:143]\n\t" \
  "v_mfma_f32_16x16x32_bf16 v[156:159], v[100:103], v[92:95], v[156:159]\n\t" \
  "v_mfma_f32_16x16x32_bf16 v[152:155], v[100:103], v[88:91], v[152:155]\n\t" \
  "v_mfma_f32_16x16x32_bf16 v[148:151], v[100:103], v[84:87], v[148:151]\n\t" \
  "v_mfma_f32_16x16x32_bf16 v[144:147], v[100:103], v[80:83], v[144:147]\n\t" \
  "s_nop 5\n\t" \
  "v_pk_mul_f32 v[140:141], v[140:141], %[qs]\n\t" \
  "v_pk_mul_f32 v[142:143], v[142:143], %[qs]\n\t" \
  "v_pk_mul_f32 v[156:157], v[156:157], %[qs]\n\t" \
  "v_pk_mul_f32 v[158:159], v[158:159], %[qs]\n\t" \
  "v_cvt_pk_bf16_f32 v44, v140, v141\n\t" \
  "v_cvt_pk_bf16_f32 v45, v142, v143\n\t" \
  "v_cvt_pk_bf16_f32 v46, v156, v157\n\t" \
  "v_cvt_pk_bf16_f32 v47, v158, v159\n\t" \
  "ds_write_b128 %[qa], v[44:47] offset:3072\n\t" \
  "ds_write_b128 %[lx], v[124:127] offset:5120\n\t" \
  "v_pk_mul_f32 v[136:137], v[136:137], %[qs]\n\t" \
  "v_pk_mul_f32 v[138:139], v[138:139], %[qs]\n\t" \
  "v_pk_mul_f32 v[152:153], v[152:153], %[qs]\n\t" \
  "v_pk_mul_f32 v[154:155], v[154:155], %[qs]\n\t" \
  "v_cvt_pk_bf16_f32 v40, v136, v137\n\t" \
  "v_cvt_pk_bf16_f32 v41, v138, v139\n\t" \
  "v_cvt_pk_bf16_f32 v42, v152, v153\n\t" \
  "v_cvt_pk_bf16_f32 v43, v154, v155\n\t" \
  "ds_write_b128 %[qa], v[40:43] offset:2048\n\t" \
  "ds_write_b128 %[lx], v[120:123] offset:4096\n\t" \
  "v_pk_mul_f32 v[132:133], v[132:133], %[qs]\n\t" \
  "v_pk_mul_f32 v[134:135], v[134:135], %[qs]\n\t" \
  "v_pk_mul_f32 v[148:149], v[148:149], %[qs]\n\t" \
  "v_pk_mul_f32 v[150:151], v[150:151], %[qs]\n\t" \
  "v_cvt_pk_bf16_f32 v36, v132, v133\n\t" \
  "v_cvt_pk_bf16_f32 v37, v134, v135\n\t" \
  "v_cvt_pk_bf16_f32 v38, v148, v149\n\t" \
  "v_cvt_pk_bf16_f32 v39, v150, v151\n\t" \
  "ds_write_b128 %[qa], v[36:39] offset:1024\n\t" \
  "ds_write_b128 %[lx], v[116:119] offset:1024\n\t" \
  "v_pk_mul_f32 v[128:129], v[128:129], %[qs]\n\t" \
  "v_pk_mul_f32 v[130:131], v[130:131], %[qs]\n\t" \
  "v_pk_mul_f32 v[144:145], v[144:145], %[qs]\n\t" \
  "v_pk_mul_f32 v[146:147], v[146:147], %[qs]\n\t" \
  "v_cvt_pk_bf16_f32 v32, v128, v129\n\t" \
  "v_cvt_pk_bf16_f32 v33, v130, v131\n\t" \
  "v_cvt_pk_bf16_f32 v34, v144, v145\n\t" \
  "v_cvt_pk_bf16_f32 v35, v146, v147\n\t" \
  "ds_write_b128 %[qa], v[32:35] offset:0\n\t" \
  "ds_write_b128 %[lx], v[112:115] offset:0\n\t" \
  "s_nop 1\n\t"
#define QKV_CLOB "v32","v33","v34","v35","v36","v37","v38","v39","v40","v41","v42","v43","v44","v45","v46","v47","v48","v49","v50","v51","v52","v53","v54","v55","v56","v57","v58","v59","v60","v61","v62","v63","v64","v65","v66","v67","v68","v69","v70","v71","v72","v73","v74","v75","v76","v77","v78","v79","v80","v81","v82","v83","v84","v85","v86","v87","v88","v89","v90","v91","v92","v93","v94","v95","v96","v97","v98","v99","v100","v101","v102","v103","v104","v105","v106","v107","v108","v109","v110","v111","v112","v113","v114","v115","v116","v117","v118","v119","v120","v121","v122","v123","v124","v125","v126","v127","v128","v129","v130","v131","v132","v133","v134","v135","v136","v137","v138","v139","v140","v141","v142","v143","v144","v145","v146","v147","v148","v149","v150","v151","v152","v153","v154","v155","v156","v157","v158","v159","v176","v177","v178","v179","v180","v181","v182","v183","v184","v185","v186","v187","v188","v189","v190","v191","v192","v193","v194","v195","v196","v197","v198","v199","v200","v201","v202","v203","v204","v205","v206","v207","v208","v209","v210","v211","v212","v213","v214","v215","v216","v217","v218","v219","v220","v221","v222","v223","scc"

#include <type_traits>
typedef unsigned short u16;
typedef unsigned long long u64;
typedef unsigned v4u __attribute__((ext_vector_type(4)));
typedef unsigned v2u __attribute__((ext_vector_type(2)));
typedef unsigned v16u __attribute__((ext_vector_type(16)));
typedef short s16x4 __attribute__((ext_vector_type(4)));
typedef short bf16x8 __attribute__((ext_vector_type(8)));
typedef __bf16 bf2_t __attribute__((ext_vector_type(2)));
typedef float f32x2 __attribute__((ext_vector_type(2)));
typedef float f32x4 __attribute__((ext_vector_type(4)));
typedef float f32x16 __attribute__((ext_vector_type(16)));
#define GAS __attribute__((address_space(1)))
#define LAS __attribute__((address_space(3)))
#define DI __device__ __forceinline__
#define RLX_AGENT __ATOMIC_RELAXED, __HIP_MEMORY_SCOPE_AGENT

#define LEADD 2
#define KROT 0
#define CODEPF 1
#define FORCE_EXACT 0
#define HALODMA 0
#define PFD 2
constexpr int STAGE = 40960;
constexpr int LDS_BYTES = 163840;
constexpr unsigned WS_F1 = 4096, WS_F3 = 20480, WS_F4 = 36864, WS_F2 = 131072;
constexpr unsigned WS_KVT = 1u << 20, WS_KVX = 5u << 20, WS_OX = 13u << 20, WS_YT = 17u << 20, WS_END = 21u << 20;
constexpr unsigned WS_STAMP = 24u << 20;
constexpr unsigned L_XQ = 98304, XQ_STR = 1168;
constexpr unsigned L_H = 122880, H_STR = 4128, HW_STR = 9216;
constexpr unsigned L_RED = 65536, L_SINK = 161792, L_WE = 98304;

DI f32x16 mfma32(v4u a, v4u b, f32x16 c) { return __builtin_amdgcn_mfma_f32_32x32x16_bf16(__builtin_bit_cast(bf16x8, a), __builtin_bit_cast(bf16x8, b), c, 0, 0, 0); }
DI f32x4 mfma16(v4u a, v4u b, f32x4 c) { return __builtin_amdgcn_mfma_f32_16x16x32_bf16(__builtin_bit_cast(bf16x8, a), __builtin_bit_cast(bf16x8, b), c, 0, 0, 0); }
DI unsigned pk2(float lo, float hi) { f32x2 v = {lo, hi}; return __builtin_bit_cast(unsigned, __builtin_convertvector(v, bf2_t)); }
DI float blo(unsigned u) { return __uint_as_float(u << 16); }
DI float bhi(unsigned u) { return __uint_as_float(u & 0xffff0000u); }
DI v4u lds_r128(unsigned a) { return *(LAS const v4u*)(size_t)a; }
DI v2u lds_r64(unsigned a) { return *(LAS const v2u*)(size_t)a; }
DI void lds_w128(unsigned a, v4u v) { *(LAS v4u*)(size_t)a = v; }
DI void lds_w64(unsigned a, v2u v) { *(LAS v2u*)(size_t)a = v; }
DI v2u lds_tr(unsigned a) { return __builtin_bit_cast(v2u, __builtin_amdgcn_ds_read_tr16_b64_v4i16((LAS s16x4*)(size_t)a)); }
DI v4u lds_tr2(unsigned a0, unsigned a1) { v2u l = lds_tr(a0), h = lds_tr(a1); return (v4u){l.x, l.y, h.x, h.y}; }
template <int OFF, int NT> DI void glds16(const void* sbase, unsigned voff, unsigned lds_dst) {
  if (NT) asm volatile("s_mov_b32 m0, %2\n\ts_nop 0\n\tglobal_load_lds_dwordx4 %0, %1 offset:%c3 nt" ::"v"(voff), "s"(sbase), "s"(lds_dst), "i"(OFF) : "memory");
  else asm volatile("s_mov_b32 m0, %2\n\ts_nop 0\n\tglobal_load_lds_dwordx4 %0, %1 offset:%c3" ::"v"(voff), "s"(sbase), "s"(lds_dst), "i"(OFF) : "memory");
}
DI void glds4(const void* sbase, unsigned voff, unsigned lds_dst) {
  asm volatile("s_mov_b32 m0, %2\n\ts_nop 0\n\tglobal_load_lds_dword %0, %1" ::"v"(voff), "s"(sbase), "s"(lds_dst) : "memory");
}
DI void wait_vm0() { asm volatile("s_waitcnt vmcnt(0)" ::: "memory"); }
DI void wait_lgkm0() { asm volatile("s_waitcnt lgkmcnt(0)" ::: "memory"); }
DI void bar() { asm volatile("s_waitcnt lgkmcnt(0)\n\ts_barrier" ::: "memory"); }
DI void wait_flags(const char* ws, unsigned off, u64 tag) {
  GAS u64* p = (GAS u64*)(ws + off);
#pragma unroll 1
  for (int spins = 0; spins < (1 << 16); ++spins) {
    u64 v = __hip_atomic_load(p, RLX_AGENT);
    if (__all(v == tag)) break;
    __builtin_amdgcn_s_sleep(2);
  }
  __builtin_amdgcn_fence(__ATOMIC_ACQUIRE, "wavefront");
  asm volatile("" ::: "memory");
}
DI v4u ld_sc1(__amdgpu_buffer_rsrc_t r, unsigned off) { return __builtin_amdgcn_raw_buffer_load_b128(r, off, 0, 16); }
DI void st_sc1(__amdgpu_buffer_rsrc_t r, unsigned off, v4u v) { __builtin_amdgcn_raw_buffer_store_b128(v, r, off, 0, 16); }
DI u64 peek(const char* ws, unsigned off) { return __hip_atomic_load((GAS u64*)(ws + off), RLX_AGENT); }
DI void confirm(const char* ws, unsigned off, u64 seen, u64 tag) {
  if (!__all(seen == tag)) wait_flags(ws, off, tag);
  __builtin_amdgcn_fence(__ATOMIC_ACQUIRE, "wavefront");
  asm volatile("" ::: "memory");
}
DI void set_flag(char* ws, unsigned off, u64 tag, int lane) {
  wait_vm0();
  if (lane == 0) __hip_atomic_store((GAS u64*)(ws + off), tag, RLX_AGENT);
}
#define STAMP(i) do {} while (0)
#define DIAG_END do {} while (0)

DI u64 load_tag(const char* ws) {
  u64 t;
  asm volatile("s_load_dwordx2 %0, %1, 0x0\n\ts_waitcnt lgkmcnt(0)" : "=s"(t) : "s"(ws) : "memory");
  return t * 0x9E3779B97F4A7C15ull + 0x632BE59BD9B4E019ull;
}
__global__ __launch_bounds__(512, 2) void fused_kernel(const u16* Aq_, const u16* Ak_, const u16* Av_, const u16* shk_, const u16* hk_, const u16* Dv_,
                                                       const u16* x_, const u16* wqkv_, const u16* wo_, u16* out_, char* ws_) {
  v16u ka0; v4u ka1; v2u ka2;
  asm volatile("s_load_dwordx16 %0, %3, 0x0\n\ts_load_dwordx4 %1, %3, 0x40\n\ts_load_dwordx2 %2, %3, 0x50"
               : "=&s"(ka0), "=&s"(ka1), "=&s"(ka2) : "s"(__builtin_amdgcn_kernarg_segment_ptr()));
  extern __shared__ __attribute__((aligned(16))) char smem[];
  const int bid = blockIdx.x, xcd = bid & 7, idx = bid >> 3;
  const int rb = (xcd & 1) * 8 + (idx & 7), hp = (xcd >> 1) * 4 + (idx >> 3);
  const int rb2 = (xcd >> 1) * 4 + (idx & 3), cs = (xcd & 1) * 8 + (idx >> 2), rot = (2 * (idx >> 2) + 4 * (idx & 3)) & 15;
  const int tid = threadIdx.x, wave = __builtin_amdgcn_readfirstlane(tid >> 6), lane = tid & 63;
  const unsigned lb = (unsigned)(size_t)(LAS char*)smem;
  u64 pc = 0; unsigned clen = 0;
  if (CODEPF) {
    unsigned lh, lm;
    asm volatile("s_getpc_b64 %0\n.Lfk_here:\n\ts_mov_b32 %1, .Lfk_end_h-.Lfk_here\n\ts_mov_b32 %2, .Lfk_end_m-.Lfk_here" : "=s"(pc), "=s"(lh), "=s"(lm));
    clen = max(lh, lm);
    glds4((const void*)pc, min((unsigned)((idx * 16 + wave * 2 + (lane & 1)) * 128), clen), lb + L_SINK + wave * 256);
  }
  const int c = lane & 15, g = lane >> 4, q4 = (lane & 15) >> 2, p4 = lane & 3;
  STAMP(0);
  const int lw = wave & 3;
  const unsigned gx = WS_KVX + (unsigned)((rb * 16 + hp) * 32768 + lw * 8192 + lane * 16);
  const unsigned lx = lb + (lw >> 1) * 49152 + (8 + 2 * (lw & 1)) * 4096 + lane * 16;
  const int r32 = lane & 31, h32 = lane >> 5;
  v4u mk0, mk1;
#define ATT_MASKS() do { _Pragma("unroll") for (int j = 0; j < 8; ++j) { \
      const int key = 16 * ((j >> 1) & 1) + 8 * (j >> 2) + 2 * (j & 1) + 4 * h32; \
      const unsigned mk = (key <= r32 ? 0xffffu : 0u) | (key + 1 <= r32 ? 0xffff0000u : 0u); \
      if (j < 4) mk0[j] = mk; else mk1[j - 4] = mk; } \
    asm volatile("" : "+{v[244:247]}"(mk0), "+{v[248:251]}"(mk1)); } while (0)

  u64 tag;
  const unsigned arow = (unsigned)(rb * 128 + lw * 32 + (lane >> 3) * 4);
  const int hpa = hp ^ lw;
  const unsigned acol = (unsigned)(hpa * 64 + (lane & 7) * 8);
  const unsigned kst = lb + L_H + lw * HW_STR;
  const unsigned f2own = WS_F2 + (unsigned)(((hp * 16 + rb) * 4 + lw) * 128);
  const int T2 = rb2 * 4 + lw;

  asm volatile("s_waitcnt lgkmcnt(0)" : "+s"(ka0), "+s"(ka1), "+s"(ka2) : "v"(acol), "s"(kst));
#define KARG(T, lo, hi) ((T)(((u64)(hi) << 32) | (lo)))
  const u16* Aq = KARG(const u16*, ka0[0], ka0[1]); const u16* Ak = KARG(const u16*, ka0[2], ka0[3]); const u16* Av = KARG(const u16*, ka0[4], ka0[5]);
  const u16* shk = KARG(const u16*, ka0[6], ka0[7]); const u16* hk = KARG(const u16*, ka0[8], ka0[9]); const u16* Dv = KARG(const u16*, ka0[10], ka0[11]);
  const u16* x = KARG(const u16*, ka0[12], ka0[13]); const u16* wqkv = KARG(const u16*, ka0[14], ka0[15]); const u16* wo = KARG(const u16*, ka1[0], ka1[1]);
  u16* out = KARG(u16*, ka1[2], ka1[3]); char* ws = KARG(char*, ka2[0], ka2[1]);
  const __amdgpu_buffer_rsrc_t wsr = __builtin_amdgcn_make_buffer_rsrc(ws, 0, WS_END, 0x00020000);
  const __amdgpu_buffer_rsrc_t outr = __builtin_amdgcn_make_buffer_rsrc(out, 0, 2048 * 2048 * 2, 0x00020000);

  v4u q0, q1;
  auto read_q = [&](int n) {
    const unsigned qb = lb + n * 49152 + lw * 2048 + (unsigned)((r32 >> 4) * 1024 + h32 * 256 + (r32 & 15) * 16);
    q0 = lds_r128(qb); q1 = lds_r128(qb + 512);
    if (n) asm volatile("s_waitcnt lgkmcnt(0)\n\ts_barrier" : "+v"(q0), "+v"(q1) :: "memory");
  };
  auto wo_stage = [&](int ln, int pos) {
    const unsigned wso = (unsigned)((ln >> 3) * 2048 + cs * 128 + (((ln & 7) ^ (((ln >> 4) & 3) << 1)) * 16));
    const char* wb = (const char*)wo + (size_t)(((pos + rot) & 15) * 2 + (pos >> 4)) * 65536;
#pragma unroll
    for (int j = 0; j < 4; ++j) glds16<0, 0>(wb + j * 16384, wso, lb + (pos < 16 ? L_WE : 0) + (pos & 15) * 4096 + j * 1024);
  };
  f32x16 acc0, acc1;
  v4u kvp[4];

  const unsigned xoe = (unsigned)((rb * 128 + lw * 32 + (lane >> 3)) * 2048 + (((lane & 7) ^ (lane >> 4)) * 16));
  const unsigned xoo = (unsigned)((rb * 128 + lw * 32 + 8 + (lane >> 3)) * 2048 + (((lane & 7) ^ (4 + (lane >> 4))) * 16));
  const unsigned woe = (unsigned)((lw * 16 + (lane >> 3)) * 6144 + hp * 128 + (((lane & 7) ^ (((lane >> 4) & 1) << 2)) * 16));
  const unsigned woo = (unsigned)((lw * 16 + 8 + (lane >> 3)) * 6144 + hp * 128 + (((lane & 7) ^ (((lane >> 4) & 1) << 2) ^ 2) * 16));
  const int pl = lane < 24 ? lane : 23;
  const unsigned pfo = (unsigned)(((idx & 7) * 8 + lw * 2) * 6144 + (pl >> 2) * 2048 + (hp & 12) * 128 + (pl & 3) * 128);
  const int lam = ((idx & 7) + (idx >> 3)) & 3;
  const u64 ktab = lam == 0 ? 0xfedbac9768532410ull : lam == 1 ? 0xfecbda8796435201ull : lam == 2 ? 0xfdceb98a75463102ull : 0xedfca9b865742103ull;
  const int krot = KROT == 1 ? (((xcd >> 1) + 2 * (xcd & 1)) & 3) * 4 : KROT == 2 ? (((xcd >> 1) ^ xcd) & 1) * 8 : 0;
  auto stage = [&](int t, int slot) {
    const int k0 = (int)((ktab >> (4 * t)) & 15), kk = (k0 + krot) & 15;
    const unsigned sb = lb + slot * STAGE;
    const char* xb = (const char*)x + kk * 128;
    const char* wb = (const char*)wqkv + (size_t)kk * 64 * 6144;
    if ((k0 & 3) == lam && k0 >= 4 - PFD && k0 < 16 - PFD) glds4((const char*)wqkv + (size_t)((kk + PFD) & 15) * 64 * 6144, pfo, lb + L_SINK + lw * 256);
    glds16<0, 0>(xb, xoe, sb + lw * 4096);
    glds16<0, 0>(xb, xoo, sb + lw * 4096 + 1024);
    glds16<0, 0>(xb + 32768, xoe, sb + lw * 4096 + 2048);
    glds16<0, 0>(xb + 32768, xoo, sb + lw * 4096 + 3072);
    glds16<0, 0>(wb, woe, sb + 16384 + lw * 2048);
    glds16<0, 0>(wb, woo, sb + 16384 + lw * 2048 + 1024);
    glds16<2048, 0>(wb, woe, sb + 16384 + 8192 + lw * 2048 - 2048);
    glds16<2048, 0>(wb, woo, sb + 16384 + 8192 + lw * 2048 + 1024 - 2048);
    glds16<0, 0>(wb + 4096, woe, sb + 16384 + 16384 + lw * 2048);
    glds16<0, 0>(wb + 4096, woo, sb + 16384 + 16384 + lw * 2048 + 1024);
  };
  if (wave >= 4) {
    glds4((const char*)wqkv + (size_t)(lam + krot) * 64 * 6144, pfo, lb + L_SINK + lw * 256);
    stage(0, 0);
    STAMP(13);
  } else {
    const int rb0 = rb * 128 + lw * 32, r0 = rb0 - 8 + (lane >> 3);
    const unsigned o1 = (unsigned)((r0 + 8) * 2048) + acol * 2;
    const bool tap = lane < 32;
#pragma unroll
    for (int i = 0; i < 4; ++i) glds16<0, 0>((const char*)Ak + i * 16384, o1, kst + 1024 + i * 1024);
    {
      const char* p = tap ? (const char*)shk + hpa * 512 + lane * 16 : lane < 40 ? (const char*)Dv + acol * 2 : (const char*)Ak + (size_t)(r0 < 0 ? 0 : r0) * 2048 + acol * 2;
      asm volatile("s_mov_b32 m0, %1\n\ts_nop 0\n\tglobal_load_lds_dwordx4 %0, off" :: "v"(p), "s"(kst) : "memory");
    }
#pragma unroll
    for (int i = 0; i < 4; ++i) glds16<0, 0>((const char*)Av + i * 16384, o1, kst + 5120 + i * 1024);
  }
  int wave2 = wave; asm volatile("" : "+s"(wave2));
  if (wave2 >= 4) {
    stage(1, 1);
    STAMP(14);
    tag = load_tag(ws);
    int slot = 2;
    const unsigned f1 = WS_F1 + (unsigned)(((hp * 16 + 4 * lw) * 4 + c) * 8);
    const unsigned ksrc = WS_KVT + (unsigned)(((hp * 16 + rb) * 16 + 4 * lw) * 1024 + lane * 16);
    const unsigned hsrc = (unsigned)((hp * 64 + rb * 4 + lw) * 4096 + lane * 16);
    v16u kv16;
    u64 f1v;
    int t = 0;
#define STEPS(END) _Pragma("unroll 1") for (; t < (END); ++t) { asm volatile("s_waitcnt vmcnt(10)\n\ts_barrier" ::: "memory"); stage(t + 2, slot); slot = slot == 2 ? 0 : slot + 1; }
#define ONE() do { stage(t + 2, slot); slot = slot == 2 ? 0 : slot + 1; ++t; } while (0)
    STEPS(lam + 4)
    asm volatile("s_waitcnt vmcnt(10)\n\ts_barrier" ::: "memory");
#pragma unroll
    for (int i = 0; i < 4; ++i) glds16<0, 0>(hk, hsrc + i * 1024, kst + 32 + i * 1024);
    ONE();
    STEPS(lam + 6)
    asm volatile("s_waitcnt vmcnt(10)\n\ts_barrier\n\tglobal_load_dwordx2 v[96:97], %1, %2 sc1" : "=&{v[96:97]}"(f1v) : "v"(f1), "s"(ws) : "memory");
    ONE();
    STEPS(lam + 8)
    asm volatile("" : "+{v[96:97]}"(f1v));
    const bool early = __all(f1v == tag);
    asm volatile("s_waitcnt vmcnt(10)\n\ts_barrier\n\t"
                 "buffer_load_dwordx4 v[80:83], %1, %2, 0 offen sc1\n\tbuffer_load_dwordx4 v[84:87], %1, %2, 0 offen offset:1024 sc1\n\t"
                 "buffer_load_dwordx4 v[88:91], %1, %2, 0 offen offset:2048 sc1\n\tbuffer_load_dwordx4 v[92:95], %1, %2, 0 offen offset:3072 sc1"
                 : "=&{v[80:95]}"(kv16) : "v"(ksrc), "s"(wsr) : "memory");
    ONE();
    STEPS(14)
    u64 f1w = tag;
    if (!early) f1w = peek(ws, f1);
    asm volatile("s_waitcnt vmcnt(10)\n\ts_barrier" : "+{v[80:95]}"(kv16) :: "memory");
    {
#pragma unroll
      for (int i = 0; i < 4; ++i) glds16<0, 0>(hk, (i == 0 && lane == 0) ? hsrc : hsrc + i * 1024 - 4, kst + H_STR + 32 + i * 1024);
      if (CODEPF) glds4((const void*)pc, min((unsigned)((idx * 16 + lw * 4 + (lane & 3)) * 128), clen), lb + L_SINK + lw * 256);
      asm volatile("s_waitcnt vmcnt(4)\n\ts_barrier" ::: "memory");
      v4u kt[4];
#pragma unroll
      for (int i = 0; i < 4; ++i) kt[i] = (v4u){kv16[4 * i], kv16[4 * i + 1], kv16[4 * i + 2], kv16[4 * i + 3]};
      const bool second = !early && __all(f1w == tag);
      asm volatile("" ::: "memory");
      if (second) {
#pragma unroll
        for (int i = 0; i < 4; ++i) kt[i] = ld_sc1(wsr, ksrc + i * 1024);
      } else {
        wait_vm0();
      }
      asm volatile("" : "+v"(kt[0]), "+v"(kt[1]), "+v"(kt[2]), "+v"(kt[3]) :: "memory");
      if (lane < 4) lds_w128(kst + (lane >> 1) * H_STR + (lane & 1) * 16, (v4u){0, 0, 0, 0});
      if (lane == 0) { v4u f = lds_r128(kst + H_STR + 32); lds_w128(kst + H_STR + 32, (v4u){0, f.x, f.y, f.z}); }
      asm volatile("s_barrier" ::: "memory");
      STAMP(1);
      if (!early && !second) {
        wait_flags(ws, f1, tag);
#pragma unroll
        for (int i = 0; i < 4; ++i) kt[i] = ld_sc1(wsr, ksrc + i * 1024);
      }
      STAMP(2);
      if (lane < 32) lds_w128(lb + L_XQ + (4 * lw + (lane >> 3)) * XQ_STR + 1024 + (lane & 7) * 16, (v4u){0, 0, 0, 0});
      const unsigned xd = lb + L_XQ + (unsigned)((p4 + 4 * q4) * XQ_STR + (480 - 128 * lw + 8 * g) * 2);
#pragma unroll
      for (int i = 0; i < 4; ++i) lds_w128(xd - i * 64, kt[i]);
      bar();
    }
    read_q(1);
    ATT_MASKS();
    STAMP(3);
    if (bid == 0 && wave == 4) {
#pragma unroll 1
      for (int i = 0; i < 4; ++i) wait_flags(ws, WS_F1 + (unsigned)((i * 64 + lane) * 32), tag);
      if (lane == 0) __hip_atomic_store((GAS u64*)ws, tag, RLX_AGENT);
    }
    {
      const unsigned s1 = rb >= 1 ? gx - 16 * 32768 : gx, s2 = rb >= 2 ? gx - 32 * 32768 : s1;
      wait_flags(ws, f2own - (rb >= 2 ? 1 + (lane & 1) : rb) * 512, tag);
      STAMP(4);
      v4u t[16];
#pragma unroll
      for (int i = 0; i < 16; ++i) t[i] = ld_sc1(wsr, (i < 8 ? s1 : s2) + (i & 7) * 1024);
      if (rb < 2) {
#pragma unroll
        for (int i = 0; i < 16; ++i) if (i >= 8 || rb == 0) t[i] = (v4u){0, 0, 0, 0};
        STAMP(5);
    }
#pragma unroll
      for (int i = 0; i < 16; ++i) lds_w128(lx - (i < 8 ? 16384 : 32768) + (i & 7) * 1024, t[i]);
      STAMP(5);
    }
#pragma unroll
    for (int i = 0; i < 4; ++i) kvp[i] = (v4u){0, 0, 0, 0};
  } else {
    ATT_MASKS();
    tag = load_tag(ws);
    asm volatile("s_waitcnt vmcnt(4)" ::: "memory");
    STAMP(13);
    {
      v4u kr[7], sk[4], vv[4];
      const unsigned ka = kst + ((lane >> 3) * 4 + 5) * 128 + (lane & 7) * 16;
#pragma unroll
      for (int i = 0; i < 4; ++i) sk[i] = lds_r128(kst + (lane & 7) * 64 + i * 16);
#pragma unroll
      for (int i = 0; i < 7; ++i) kr[i] = lds_r128(ka + i * 128);
      const v4u dd = lds_r128(kst + 512 + (lane & 7) * 16);
      if (arow == 0) { kr[0] = kr[1] = kr[2] = (v4u){0, 0, 0, 0}; }
      f32x2 ks[4][4];
#pragma unroll
      for (int d = 0; d < 4; ++d) {
        const f32x2 w0 = {blo(sk[d].x), blo(sk[d].z)}, w1 = {bhi(sk[d].x), bhi(sk[d].z)}, w2 = {blo(sk[d].y), blo(sk[d].w)}, w3 = {bhi(sk[d].y), bhi(sk[d].w)};
        f32x2 kf[7];
#pragma unroll
        for (int i = 0; i < 7; ++i) kf[i] = (f32x2){blo(kr[i][d]), bhi(kr[i][d])};
#pragma unroll
        for (int i = 0; i < 4; ++i) ks[i][d] = kf[i] * w3 + kf[i + 1] * w2 + kf[i + 2] * w1 + kf[i + 3] * w0;
        if (d == 1) {
          wait_vm0();
#pragma unroll
          for (int i = 0; i < 4; ++i) vv[i] = lds_r128(ka + 4480 + i * 128);
        }
      }
#pragma unroll
      for (int i = 0; i < 4; ++i) {
#pragma unroll
        for (int d = 0; d < 4; ++d) kvp[i][d] = pk2(ks[i][d].x * blo(vv[i][d]), ks[i][d].y * bhi(vv[i][d]));
        lds_w128(kst + ((lane >> 3) * 4 + i) * 144 + (lane & 7) * 16, kvp[i]);
      }
      asm volatile("" ::: "memory");
      const unsigned ta = kst + (4 * (7 - q4) + p4) * 144 + g * 8;
#pragma unroll
      for (int i = 0; i < 4; ++i)
        st_sc1(wsr, WS_KVT + (unsigned)(((hpa * 16 + i * 4 + g) * 16 + rb) * 1024 + (3 - lw) * 256 + c * 16), lds_tr2(ta + i * 32, ta + i * 32 - 16 * 144));
#pragma unroll
      for (int i = 0; i < 4; ++i)
#pragma unroll
        for (int d = 0; d < 4; ++d) kvp[i][d] = pk2(blo(kvp[i][d]) * blo(dd[d]), bhi(kvp[i][d]) * bhi(dd[d]));
      asm volatile("" : "+v"(kvp[0]), "+v"(kvp[1]), "+v"(kvp[2]), "+v"(kvp[3]));
    }
    STAMP(14);
    const int mh = wave & 1, n = wave >> 1;
    unsigned x0 = lb + (mh * 64 + c) * 128 + 16 * (g ^ (c >> 1));
    unsigned x1 = x0 ^ 64;
    unsigned w0 = lb + 16384 + (8 * g + q4) * 128 + (((2 * n) ^ (2 * ((q4 >> 1) & 1)) ^ (g & 1)) * 32) + 8 * p4;
    unsigned w1 = w0 ^ 32;
    int sl = 0, sd = 0, cnt = 12;
    const int l2 = lane;
    unsigned ha = kst + 8 + (8 * (l2 >> 4) + ((l2 & 15) >> 2) + (l2 & 3)) * 8;
    unsigned xa = lb + L_XQ + wave * XQ_STR + (60 - 4 * (l2 & 15) + 8 * (l2 >> 4)) * 2;
    unsigned yo = WS_YT + (unsigned)((hp * 16 + (l2 >> 4)) * 16384 + ((l2 >> 2) & 3) * 4096 + (rb * 4 + wave) * 64 + (l2 & 3) * 16);
    int src2 = (((l2 & 31) >> 1) + 16 * ((l2 >> 5) + 2 * (l2 & 1))) * 4;
    int srcl = (l2 >= 16 ? l2 - 16 : (l2 + 47 + (l2 == 0 ? 16 : 0))) * 4;
    unsigned gx1 = gx + 4096, qrd = lb + lw * 2048 + (unsigned)((r32 >> 4) * 1024 + h32 * 256 + (r32 & 15) * 16);
    asm volatile("" : "+v"(ha), "+v"(xa), "+v"(yo), "+v"(src2), "+v"(srcl), "+v"(gx1), "+v"(qrd));
    v16u vf;
    constexpr float QS = 0.17677669529663687f * 1.4426950408889634f;
    __builtin_amdgcn_s_setprio(2);
    asm volatile(QKV_LOOP
                 : "=&{v[160:175]}"(vf),
                   [x0] "+v"(x0), [x1] "+v"(x1), [w0] "+v"(w0), [w1] "+v"(w1), [sl] "+s"(sl), [sd] "+s"(sd), [cnt] "+s"(cnt)
                 : [cpos] "s"(40960), [cneg] "s"(-81920), [fo] "v"(WS_F1 + (unsigned)(((hpa * 16 + rb) * 4 + lw) * 8)), [tg] "v"(tag), [wsb] "s"(ws),
                   [qs] "s"((f32x2){QS, QS}), [qa] "v"(lb + n * 49152 + mh * 4096 + lane * 16), [g0] "v"(gx), [g1] "v"(gx1), [lx] "v"(lx), [rs] "s"(wsr)
                 : QKV_CLOB, "memory");
    STAMP(1);
    bar();
    q0 = lds_r128(qrd); q1 = lds_r128(qrd + 512);
    STAMP(3);
    asm volatile(CONV_ASM
                 : [yo] "+v"(yo), "+{v[160:175]}"(vf)
                 : [lx] "v"(lx), [ha] "v"(ha), [xa] "v"(xa), [wsb] "s"(ws), [fo] "v"(f2own), [tg] "v"(tag), [sl] "v"(srcl), [s2] "v"(src2), [l0] "s"(1ull), [rs] "s"(wsr)
                 : CONV_CLOB, "memory");
    STAMP(4);
    __builtin_amdgcn_s_setprio(1);
  }
  const int par = wave >> 2;
  u64 f4v;
  f32x16 sb;
  unsigned wpf;
  {
    int la = lane; asm volatile("" : "+v"(la));
    const int r32 = la & 31, h32 = la >> 5;
    const unsigned fo = (unsigned)((r32 >> 4) * 1024 + h32 * 256 + (r32 & 15) * 16);
    const unsigned ko = (unsigned)(((r32 >> 3) & 1) * 1024 + h32 * 256 + ((r32 & 7) | ((r32 >> 4) << 3)) * 16);
    const unsigned kvb = lb + par * 49152 + lw * 4096;
    const unsigned ka = kvb + ko, va = kvb + 2048 + fo;
    const int i0 = __builtin_amdgcn_readfirstlane(8 - 4 * rb - lw);
    const unsigned onev = ((((la & 15) >> 2) & 1) == ((la >> 4) & 1)) ? 0x3f803f80u : 0u;
    const v4u ones = {onev, onev, onev, onev};
    const unsigned pfo2 = (unsigned)((idx * 32 + wave * 4 + (r32 >> 3)) * 2048 + (xcd & 1) * 1024 + (la & 7) * 128);
    const char* wb0 = (const char*)wo + (size_t)((2 * wave + rot) & 15) * 131072;
    const char* wb1 = (const char*)wo + (size_t)((2 * wave + 1 + rot) & 15) * 131072;
    const unsigned od = WS_OX + (unsigned)(((rb * 4 + lw) * 32 + par * 16 + hp) * 2048) + fo;
    f32x16 o;
    f32x4 ls;
    float mx;
    if (par) bar();
    STAMP(7);
    asm volatile("global_load_dword %0, %1, %2" : "=v"(wpf) : "v"(pfo2), "s"(wo) : "memory");
    asm volatile(ATT0_ASM : "=&{v[80:95]}"(sb), [mx] "=&v"(mx) : [ka] "v"(ka), [q0] "v"(q0), [q1] "v"(q1), [i0] "s"(i0) : ATT1_CLOB, "memory");
    STAMP(6);
#pragma unroll 1
    for (int pass = 0;; ++pass) {
      unsigned wm = lb + L_WE + wave * 8192, wv = (unsigned)((la >> 3) * 2048 + cs * 128 + (((la & 7) ^ (((la >> 4) & 3) << 1)) * 16));
      asm volatile(ATT2_ASM : "=&{v[112:127]}"(o), "=&{v[128:131]}"(ls), "+{v[80:95]}"(sb), [wm] "+s"(wm), [wv] "+v"(wv), [pk] "=&v"(f4v)
                   : [wb0] "s"(wb0), [wb1] "s"(wb1), [pko] "v"(WS_F4 + (unsigned)(hpa * 512 + la * 8)), [wsb] "s"(ws), [par] "s"(par),
                     [fo] "v"(WS_F4 + (unsigned)(((hp * 16 + rb) * 4 + lw) * 8)), [tg] "v"(tag),
                     [qo] "v"((unsigned)((rb * 128 + lw * 32 + (la >> 3) * 4) * 2048 + hpa * 128 + (la & 7) * 16)), [qb] "s"(Aq), [qb2] "s"(Aq + 2048),
                     [ka] "v"(ka), [va] "v"(va), [q0] "v"(q0), [q1] "v"(q1), [ones] "v"(ones), [mx] "v"(mx), "{v[244:247]}"(mk0), "{v[248:251]}"(mk1), [i0] "s"(i0),
                     [c0] "s"(i0 > 1 ? 0xff800000u : 0u)
                   : ATT2_CLOB, "memory");
      f32x2 big = {ls[0] * 1.0e19f, 0.f};
#pragma unroll
      for (int i = 0; i < 16; i += 4) big += (f32x2){o[i], o[i + 1]} + (f32x2){o[i + 2], o[i + 3]};
      const unsigned bb = __float_as_uint(big.x + big.y);
      if (pass || (!FORCE_EXACT && !__any((bb & 0x7f800000u) == 0x7f800000u))) break;
      asm volatile("s_waitcnt vmcnt(0)" : "+{v[80:95]}"(sb), "+v"(f4v) :: "memory");
      asm volatile(ATT1_ASM : "=&{v[80:95]}"(sb), [mx] "=&v"(mx) : [ka] "v"(ka), [q0] "v"(q0), [q1] "v"(q1), [i0] "s"(i0) : ATT1_CLOB, "memory");
    }
    const float rl = __builtin_amdgcn_rcpf(ls[0]);
#pragma unroll
    for (int s = 0; s < 2; ++s) {
      v4u u = {pk2(o[8 * s] * rl, o[8 * s + 1] * rl), pk2(o[8 * s + 2] * rl, o[8 * s + 3] * rl), pk2(o[8 * s + 4] * rl, o[8 * s + 5] * rl), pk2(o[8 * s + 6] * rl, o[8 * s + 7] * rl)};
      st_sc1(wsr, od + s * 512, u);
    }
    __builtin_amdgcn_s_setprio(0);
    STAMP(8);
  }
  {
    int ln = lane; asm volatile("" : "+v"(ln));
    GAS u64* f3own = (GAS u64*)(ws + WS_F3 + ((rb * 4 + lw) * 32 + par * 16 + hp) * 8);
    const unsigned opf = WS_OX + (unsigned)((T2 * 32 + par * 16 + rot) * 2048 + (ln & 31) * 128);
    unsigned pf1;
    if (par) {
      asm volatile("s_waitcnt vmcnt(2)\n\ts_barrier" :: "v"(wpf) : "memory");
      STAMP(9);
      wo_stage(ln, 16 + lw);
      asm volatile("s_waitcnt vmcnt(4)" ::: "memory");
      if (ln == 0) __hip_atomic_store(f3own, tag, RLX_AGENT);
      wo_stage(ln, 20 + lw); wo_stage(ln, 24 + lw); wo_stage(ln, 28 + lw);
    } else {
      asm volatile("s_waitcnt vmcnt(2)" : "+v"(f4v), "+{v[80:95]}"(sb) : "v"(wpf) : "memory");
      const bool seen = __all(f4v == tag);
      v4u yl[4];
      const unsigned ys = WS_YT + (unsigned)((hpa * 16 + rb) * 16384 + lw * 4096 + (ln >> 3) * 512 + (ln & 7) * 16);
      auto yload = [&]() {
#pragma unroll
        for (int i = 0; i < 4; ++i) asm volatile("buffer_load_dwordx4 %0, %1, %2, 0 offen offset:%c3 sc1" : "=v"(yl[i]) : "v"(ys), "s"(wsr), "i"(i * 128) : "memory");
      };
#define YPIN "+v"(yl[0]), "+v"(yl[1]), "+v"(yl[2]), "+v"(yl[3])
      if (seen) {
        yload();
        asm volatile("s_waitcnt vmcnt(4)" ::: "memory");
        if (ln == 0) __hip_atomic_store(f3own, tag, RLX_AGENT);
        asm volatile("s_barrier\n\ts_waitcnt vmcnt(1)" : YPIN :: "memory");
      } else {
        const unsigned f4 = WS_F4 + (unsigned)(hpa * 512 + ln * 8);
        u64 f4w = peek(ws, f4);
        asm volatile("" : "+v"(f4w) :: "memory");
        if (ln == 0) __hip_atomic_store(f3own, tag, RLX_AGENT);
        const bool now = __all(f4w == tag);
        if (!now) { asm volatile("s_barrier" ::: "memory"); wait_flags(ws, f4, tag); }
        yload();
        if (now) asm volatile("s_barrier" ::: "memory");
        asm volatile("s_waitcnt vmcnt(0)" : YPIN :: "memory");
      }
      STAMP(5);
      v2u yv[8];
      const int sl0 = ((ln & 7) * 8 + (ln >> 4)) * 4;
      const bool up = (ln >> 3) & 1;
#pragma unroll
      for (int j = 0; j < 8; ++j) {
        const int sl = sl0 + (j & 1) * 16;
        const unsigned a0 = __builtin_amdgcn_ds_bpermute(sl, yl[j >> 1].x), a1 = __builtin_amdgcn_ds_bpermute(sl, yl[j >> 1].y);
        const unsigned a2 = __builtin_amdgcn_ds_bpermute(sl, yl[j >> 1].z), a3 = __builtin_amdgcn_ds_bpermute(sl, yl[j >> 1].w);
        yv[j] = (v2u){up ? a2 : a0, up ? a3 : a1};
      }
#pragma unroll
      for (int i = 0; i < 4; ++i)
#pragma unroll
        for (int d = 0; d < 4; ++d) {
          const unsigned ya = yv[2 * d][i >> 1], yb = yv[2 * d + 1][i >> 1], qq = __float_as_uint(sb[4 * i + d]);
          const float y0 = (i & 1) ? bhi(ya) : blo(ya), y1 = (i & 1) ? bhi(yb) : blo(yb);
          kvp[i][d] = pk2((y0 + blo(kvp[i][d])) * blo(qq), (y1 + bhi(kvp[i][d])) * bhi(qq));
        }
      asm volatile("" : "+v"(kvp[0]), "+v"(kvp[1]), "+v"(kvp[2]), "+v"(kvp[3]));
      STAMP(9);
    }
    asm volatile("buffer_load_dword %0, %1, %2, 0 offen sc1" : "=v"(pf1) : "v"(opf), "s"(wsr) : "memory");
    const int wr = 16 * (ln >> 5) + 4 * ((ln >> 4) & 1) + ((ln & 15) >> 2);
    const unsigned wa0 = lb + (par ? 0 : L_WE) + wr * 128 + (((wr >> 1) & 3) * 32) + 8 * (ln & 3);
    int so = rot * 2048;
    const unsigned f3 = WS_F3 + (unsigned)((T2 * 32 + par * 16 + (ln & 15)) * 8);
    const u64 f3v = peek(ws, f3);
    if (par) asm volatile("s_waitcnt vmcnt(2)\n\ts_barrier" ::: "memory");
    STAMP(10);
    confirm(ws, f3, f3v, tag);
    asm volatile("" :: "v"(pf1));
    STAMP(11);
    asm volatile(OPROJ_ASM : "=&{v[64:79]}"(acc0), "=&{v[80:95]}"(acc1), [so] "+s"(so)
                 : [oa] "v"(WS_OX + (unsigned)((T2 * 32 + par * 16) * 2048 + ln * 16)), [rs] "s"(wsr), [dob] "s"(1 - par),
                   [w0] "v"(wa0), [w1] "v"(wa0 ^ 32), [w2] "v"(wa0 ^ 64), [w3] "v"(wa0 ^ 96) : OPROJ_CLOB, "memory");
    STAMP(12);
  }
  int l3 = lane; asm volatile("" : "+v"(l3));
  if (par) {
    asm volatile("s_barrier" ::: "memory");
    const unsigned ra = lb + L_RED + lw * 8192 + l3 * 16;
    const unsigned ob = lb + L_WE + lw * 4608;
#pragma unroll
    for (int i = 0; i < 8; ++i) {
      const f32x4 pr = __builtin_bit_cast(f32x4, lds_r128(ra + i * 1024));
      const f32x16& a = i < 4 ? acc0 : acc1; const int k = 4 * (i & 3);
      lds_w64(ob + ((i >> 2) * 16 + (l3 & 15)) * 144 + (i & 3) * 32 + (l3 >> 4) * 8, (v2u){pk2(a[k] + pr[0], a[k + 1] + pr[1]), pk2(a[k + 2] + pr[2], a[k + 3] + pr[3])});
    }
    wait_lgkm0();
#pragma unroll
    for (int i = 0; i < 4; ++i) {
      const v4u r = lds_r128(ob + (i * 8 + (l3 >> 3)) * 144 + (l3 & 7) * 16);
      st_sc1(outr, (unsigned)((T2 * 32 + i * 8 + (l3 >> 3)) * 2048 + 1024 + cs * 64 + (l3 & 7) * 8) * 2, r);
    }
    asm volatile(".Lfk_end_h:");
  } else {
#pragma unroll
    for (int i = 0; i < 8; ++i) {
      const f32x16& a = i < 4 ? acc0 : acc1; const int k = 4 * (i & 3);
      lds_w128(lb + L_RED + lw * 8192 + l3 * 16 + i * 1024, __builtin_bit_cast(v4u, (f32x4){a[k], a[k + 1], a[k + 2], a[k + 3]}));
    }
#pragma unroll
    for (int i = 0; i < 4; ++i) st_sc1(outr, (unsigned)((rb * 128 + lw * 32 + (l3 >> 3) * 4 + i) * 2048 + hpa * 64 + (l3 & 7) * 8) * 2, kvp[i]);
    bar();
    asm volatile(".Lfk_end_m:");
  }
  STAMP(15);
  DIAG_END;
}

extern "C" void kernel_launch(void* const* d_in, const int* in_sizes, int n_in, void* d_out, int out_size, void* d_ws, size_t ws_size,
                              hipStream_t stream) {
  (void)in_sizes; (void)n_in; (void)out_size; (void)ws_size;
  (void)hipFuncSetAttribute((const void*)fused_kernel, hipFuncAttributeMaxDynamicSharedMemorySize, LDS_BYTES);
  fused_kernel<<<256, 512, LDS_BYTES, stream>>>((const u16*)d_in[0], (const u16*)d_in[1], (const u16*)d_in[2], (const u16*)d_in[3], (const u16*)d_in[4],
                                                (const u16*)d_in[5], (const u16*)d_in[6], (const u16*)d_in[7], (const u16*)d_in[8], (u16*)d_out, (char*)d_ws);
}
```
